```python
import torch

def _softmax(x, axis=-1):
    return torch.softmax(x, dim=axis)
def _gelu(x):
    return torch.nn.functional.gelu(x, approximate="tanh")
def _silu(x):
    return torch.nn.functional.silu(x)
def _sigmoid(x):
    return torch.sigmoid(x)
def _layernorm(x, axis=-1, eps=1e-5):
    m = x.mean(dim=axis, keepdim=True)
    v = x.var(dim=axis, keepdim=True, unbiased=False)
    return (x - m) / torch.sqrt(v + eps)
def _scatter_add(tgt, idx, src, axis=0):
    return tgt.clone().index_add_(axis, idx, src)
def _segment_sum(src, seg, n):
    out = torch.zeros((n,) + src.shape[1:], dtype=src.dtype)
    return out.index_add_(0, seg, src)
def _pad(x, pw):
    flat = tuple(v for pair in reversed(pw) for v in pair)
    return torch.nn.functional.pad(x, flat)

kernel_name = 'interleave_layers__spectral_fnet__glu_gated_glu__prompt'

def setup_inputs():

    _w = (lambda a: (lambda t: t.to(torch.bfloat16) if t.is_floating_point() else t)(torch.as_tensor(a)))
    def _A_setup():
        B, S, D = 1, 2048, 576
        ns = torch.arange(S)[:, None] * torch.arange(S)[None, :]
        nd = torch.arange(D)[:, None] * torch.arange(D)[None, :]
        Ws_re = torch.cos(-2.0 * torch.pi * ns / S).to(float)
        Ws_im = torch.sin(-2.0 * torch.pi * ns / S).to(float)
        Wd_re = torch.cos(-2.0 * torch.pi * nd / D).to(float)
        Wd_im = torch.sin(-2.0 * torch.pi * nd / D).to(float)
        return {
            "x": _w((lambda *s: torch.randn(*s, dtype=torch.bfloat16) * 0.05)(B, S, D)),
            "dft_seq_re": _w(Ws_re),
            "dft_seq_im": _w(Ws_im),
            "dft_hid_re": _w(Wd_re),
            "dft_hid_im": _w(Wd_im),
        }
    _A_in = _A_setup()

    def _B_setup():
        B, S, D, F = 1, 2048, 576, 4608
        return {
            "x": _w((lambda *s: torch.randn(*s, dtype=torch.bfloat16) * 0.05)(B, S, D)),
            "w_gate": _w((lambda *s: torch.randn(*s, dtype=torch.bfloat16) * 1.6)(D, F)),
            "w_up": _w((lambda *s: torch.randn(*s, dtype=torch.bfloat16) * 0.4)(D, F)),
            "w_down": _w((lambda *s: torch.randn(*s, dtype=torch.bfloat16) * 0.4)(F, D)),
        }
    _B_in = _B_setup()

    return {
        "A_x": _A_in["x"],
        "A_dft_seq_re": _A_in["dft_seq_re"],
        "A_dft_seq_im": _A_in["dft_seq_im"],
        "A_dft_hid_re": _A_in["dft_hid_re"],
        "A_dft_hid_im": _A_in["dft_hid_im"],
        "B_x": _B_in["x"],
        "B_w_gate": _B_in["w_gate"],
        "B_w_up": _B_in["w_up"],
        "B_w_down": _B_in["w_down"],
    }

def reference(A_x, A_dft_seq_re, A_dft_seq_im, A_dft_hid_re, A_dft_hid_im, B_x, B_w_gate, B_w_up, B_w_down):
    def _A_ref(x, dft_seq_re, dft_seq_im, dft_hid_re, dft_hid_im):
        a = torch.einsum("bsd,ed->bse", x, dft_hid_re)
        b = torch.einsum("bsd,ed->bse", x, dft_hid_im)
        y = torch.einsum("ts,bse->bte", dft_seq_re, a) - torch.einsum("ts,bse->bte", dft_seq_im, b)
        return y

    def _B_ref(x, w_gate, w_up, w_down):
        g = torch.einsum("bsd,df->bsf", x, w_gate)
        u = torch.einsum("bsd,df->bsf", x, w_up)
        h = _sigmoid(g) * u
        return torch.einsum("bsf,fd->bsd", h, w_down)

    x = A_x
    x = x + _A_ref(_layernorm(x, axis=-1), A_dft_seq_re, A_dft_seq_im, A_dft_hid_re, A_dft_hid_im)
    x = x + _B_ref(_layernorm(x, axis=-1), B_w_gate, B_w_up, B_w_down)
    x = x + _A_ref(_layernorm(x, axis=-1), A_dft_seq_re, A_dft_seq_im, A_dft_hid_re, A_dft_hid_im)
    x = x + _B_ref(_layernorm(x, axis=-1), B_w_gate, B_w_up, B_w_down)
    return x
```

```cpp
#include <hip/hip_runtime.h>
#include <hip/hip_bf16.h>

typedef unsigned short u16;
typedef __attribute__((ext_vector_type(8))) short bf16x8;
typedef __attribute__((ext_vector_type(4))) short bf16x4;
typedef __attribute__((ext_vector_type(4))) float f32x4;
typedef __attribute__((ext_vector_type(2))) unsigned u32x2;
#define LAS __attribute__((address_space(3)))
#define GAS __attribute__((address_space(1)))

constexpr int S_ = 2048, D_ = 576, F_ = 4608;

enum { G1 = 1, G2 = 2, G3 = 3, G4 = 4 };

template <int KIND> struct Cfg;
template <> struct Cfg<G1> { static constexpr int BM = 64,  MT = 1, BK = 64, NK = 9,  ST = 6, MROW = 0, NMODE = 1, NMAT = 1; };
template <> struct Cfg<G2> { static constexpr int BM = 128, MT = 2, BK = 64, NK = 16, ST = 4, MROW = 1, NMODE = 0, NMAT = 1; };
template <> struct Cfg<G3> { static constexpr int BM = 256, MT = 4, BK = 32, NK = 18, ST = 4, MROW = 0, NMODE = 2, NMAT = 2; };
template <> struct Cfg<G4> { static constexpr int BM = 128, MT = 2, BK = 64, NK = 18, ST = 4, MROW = 0, NMODE = 2, NMAT = 1; };

__device__ __forceinline__ unsigned pack_bf16(float lo, float hi) {
    unsigned r;
    asm("v_cvt_pk_bf16_f32 %0, %1, %2" : "=v"(r) : "v"(lo), "v"(hi));
    return r;
}
__device__ __forceinline__ float bf_lo(unsigned v) { return __uint_as_float(v << 16); }
__device__ __forceinline__ float bf_hi(unsigned v) { return __uint_as_float(v & 0xffff0000u); }

__device__ __forceinline__ void glds16(const char* g, char* l) {
    __builtin_amdgcn_global_load_lds((const GAS unsigned*)g, (LAS unsigned*)l, 16, 0, 0);
}
template <int N> __device__ __forceinline__ void wait_vm() { asm volatile("s_waitcnt vmcnt(%0)" ::"i"(N) : "memory"); }

struct GArgs {
    const char* m0;
    const char* m1;
    const char* n0;
    const char* n1;
    char* out;
};

template <int KIND, int NT, bool TYPEB>
__device__ __forceinline__ void gemm_body(const GArgs& a, char* shm, int wid, int lane) {
    using C = Cfg<KIND>;
    constexpr int BM = C::BM, MT = C::MT, BK = C::BK, NK = C::NK, ST = C::ST, KK = BK / 32, NMAT = C::NMAT;
    constexpr int MB = BM * BK * 2;
    constexpr int NB = 18432;
    constexpr int STB = MB + NB;
    constexpr int MI = MB / 4096;
    constexpr int CNT = TYPEB ? 5 : MI;
    constexpr int NT0 = TYPEB ? 5 : 0;
    const int wm = wid & 3;

    const int bid = blockIdx.x, xcd = bid & 7, loc = bid >> 3;
    int tm, tn, ks = 0;
    if (KIND == G1) { tm = xcd * 4 + (loc >> 3); tn = loc & 7; }
    else if (KIND == G3) { tn = xcd * 4 + (loc >> 3); tm = loc & 7; }
    else { ks = xcd >> 1; tm = (xcd & 1) * 8 + (loc >> 2); tn = loc & 3; }

    const char* gbase;
    unsigned goff[CNT];
    int gstep;
    if (!TYPEB) {
        if (C::MROW) {
            gbase = (ks >= 2 ? a.m1 : a.m0) + (size_t)(tm * BM) * (S_ * 2) + (ks & 1) * (NK * 128);
            gstep = 128;
#pragma unroll
            for (int i = 0; i < CNT; ++i) {
                int row = (wm * MI + i) * 8 + (lane >> 3);
                int c = (lane & 7) ^ (row & 7);
                goff[i] = row * (S_ * 2) + c * 16;
            }
        } else {
            constexpr int NKB = (KIND == G4) ? (F_ / 32) : (D_ / 32);
            gbase = a.m0 + ((size_t)(tm * (BM / 16)) * NKB + ks * (NK * KK)) * 1024;
            gstep = KK * 1024;
#pragma unroll
            for (int i = 0; i < CNT; ++i) {
                int f = wm * MI + i;
                int kk = f / (BM / 16), mt = f % (BM / 16);
                goff[i] = (mt * NKB + kk) * 1024 + lane * 16;
            }
        }
    } else {
        if (C::NMODE == 0) {
            gbase = a.n0 + ((size_t)(tn * 9) * 128 + ks * (NK * KK)) * 1024;
            gstep = KK * 1024;
#pragma unroll
            for (int i = 0; i < CNT; ++i) {
                int o = wm * 4608 + i * 1024 + lane * 16;
                int f = o >> 10, w = o & 1023;
                int kk = f / 9, nt = f % 9;
                goff[i] = (nt * 128 + kk) * 1024 + w;
            }
        } else if (C::NMODE == 1) {
            gbase = (tn >= 4 ? a.n1 : a.n0) + (size_t)((tn & 3) * 144) * (D_ * 2);
            gstep = 128;
#pragma unroll
            for (int i = 0; i < CNT; ++i) {
                int o = wm * 4608 + i * 1024 + lane * 16;
                int row = o >> 7;
                int c = ((o >> 4) & 7) ^ (row & 7);
                goff[i] = row * (D_ * 2) + c * 16;
            }
        } else {
            constexpr int LDW = (KIND == G3) ? F_ : D_;
            const char* wsel = (NMAT == 2 && wm >= 2) ? a.n1 : a.n0;
            gbase = wsel + (size_t)(ks * (NK * BK)) * (LDW * 2) + tn * 288;
            gstep = BK * LDW * 2;
#pragma unroll
            for (int i = 0; i < CNT; ++i) {
                int o = (NMAT == 2 ? (wm & 1) : wm) * 4608 + i * 1024 + lane * 16;
                int slot = o / 288, cb = o - slot * 288;
                int kr = (slot & ~12) | ((slot & 4) << 1) | ((slot & 8) >> 1);
                goff[i] = kr * (LDW * 2) + cb;
            }
        }
    }
    char* ldst = shm + (TYPEB ? MB + wm * 4608 : wm * (MI * 1024));

    auto issue = [&](int slot_off) {
#pragma unroll
        for (int i = 0; i < CNT; ++i) {
            if (TYPEB && i == 4) {
                if (lane < 32) glds16(gbase + goff[i], ldst + slot_off + i * 1024);
            } else {
                glds16(gbase + goff[i], ldst + slot_off + i * 1024);
            }
        }
        gbase += gstep;
    };

    const int r = lane & 15, q = lane >> 4;
    int maddr[KK], naddr[KK];
#pragma unroll
    for (int kk = 0; kk < KK; ++kk) {
        if (C::MROW) maddr[kk] = (wm * MT * 16 + r) * 128 + (((kk * 4 + q) ^ (r & 7)) * 16);
        else maddr[kk] = (kk * (BM / 16) + wm * MT) * 1024 + lane * 16;
        if (C::NMODE == 0) naddr[kk] = MB + (kk * 9 + NT0) * 1024 + lane * 16;
        else if (C::NMODE == 1) naddr[kk] = MB + (NT0 * 16 + r) * 128 + (((kk * 4 + q) ^ (r & 7)) * 16);
        else naddr[kk] = MB + (kk * 32 + 16 * (q >> 1) + 4 * (q & 1) + ((lane >> 2) & 3)) * 288 + NT0 * 32 + (lane & 3) * 8;
    }

    f32x4 acc[NMAT][NT][MT];
#pragma unroll
    for (int x = 0; x < NMAT; ++x)
#pragma unroll
        for (int n = 0; n < NT; ++n)
#pragma unroll
            for (int m = 0; m < MT; ++m) acc[x][n][m] = (f32x4){0.f, 0.f, 0.f, 0.f};

    auto compute = [&](int slot_off) {
        const char* sb = shm + slot_off;
#pragma unroll
        for (int kk = 0; kk < KK; ++kk) {
            bf16x8 mf[MT];
#pragma unroll
            for (int m = 0; m < MT; ++m)
                mf[m] = *(const bf16x8*)(sb + maddr[kk] + m * (C::MROW ? 2048 : 1024));
#pragma unroll
            for (int x = 0; x < NMAT; ++x)
#pragma unroll
                for (int n = 0; n < NT; ++n) {
                    bf16x8 nf;
                    if (C::NMODE == 0) nf = *(const bf16x8*)(sb + naddr[kk] + n * 1024);
                    else if (C::NMODE == 1) nf = *(const bf16x8*)(sb + naddr[kk] + n * 2048);
                    else {
                        bf16x4 lo = __builtin_amdgcn_ds_read_tr16_b64_v4i16((LAS bf16x4*)(sb + naddr[kk] + x * 9216 + n * 32));
                        bf16x4 hi = __builtin_amdgcn_ds_read_tr16_b64_v4i16((LAS bf16x4*)(sb + naddr[kk] + x * 9216 + n * 32 + 2304));
                        nf = __builtin_shufflevector(lo, hi, 0, 1, 2, 3, 4, 5, 6, 7);
                    }
#pragma unroll
                    for (int m = 0; m < MT; ++m) {
                        if (KIND == G1) acc[x][n][m] = __builtin_amdgcn_mfma_f32_16x16x32_bf16(mf[m], nf, acc[x][n][m], 0, 0, 0);
                        else acc[x][n][m] = __builtin_amdgcn_mfma_f32_16x16x32_bf16(nf, mf[m], acc[x][n][m], 0, 0, 0);
                    }
                }
        }
    };

#pragma unroll
    for (int s = 0; s < ST - 1; ++s) issue(s * STB);
    int rd = 0, wr = (ST - 1) * STB;
    for (int kt = 0; kt < NK - (ST - 1); ++kt) {
        wait_vm<CNT*(ST - 2)>();
        __builtin_amdgcn_s_barrier();
        asm volatile("" ::: "memory");
        issue(wr);
        compute(rd);
        asm volatile("s_waitcnt lgkmcnt(0)" ::: "memory");
        wr = rd;
        rd = (rd + STB == ST * STB) ? 0 : rd + STB;
    }
#pragma unroll
    for (int t = 0; t < ST - 1; ++t) {
        if (t == 0) wait_vm<CNT*(ST - 2)>();
        if (t == 1) wait_vm<CNT*(ST - 3 > 0 ? ST - 3 : 0)>();
        if (t == 2) wait_vm<CNT*(ST - 4 > 0 ? ST - 4 : 0)>();
        if (t == 3) wait_vm<CNT*(ST - 5 > 0 ? ST - 5 : 0)>();
        if (t == 4) wait_vm<CNT*(ST - 6 > 0 ? ST - 6 : 0)>();
        __builtin_amdgcn_s_barrier();
        asm volatile("" ::: "memory");
        compute(rd);
        rd = (rd + STB == ST * STB) ? 0 : rd + STB;
    }

    if (KIND == G1) {
        const int part = tn >> 2;
        const float sg = part ? -1.f : 1.f;
#pragma unroll
        for (int n = 0; n < NT; ++n)
#pragma unroll
            for (int m = 0; m < MT; ++m) {
                int st16 = tm * (BM / 16) + wm * MT + m;
                int et = (tn & 3) * 9 + NT0 + n;
                int kb = part * 64 + (st16 >> 1);
                int g = 2 * (st16 & 1) + (q >> 1);
                u32x2 v;
                v.x = pack_bf16(acc[0][n][m][0] * sg, acc[0][n][m][1] * sg);
                v.y = pack_bf16(acc[0][n][m][2] * sg, acc[0][n][m][3] * sg);
                *(u32x2*)(a.out + ((size_t)(et * 128 + kb)) * 1024 + (g * 16 + r) * 16 + (q & 1) * 8) = v;
            }
    } else if (KIND == G3) {
#pragma unroll
        for (int n = 0; n < NT; ++n)
#pragma unroll
            for (int m = 0; m < MT; ++m) {
                int st16 = tm * (BM / 16) + wm * MT + m;
                int f16 = tn * 9 + NT0 + n;
                int kb = f16 >> 1;
                int g = 2 * (f16 & 1) + (q >> 1);
                float h[4];
#pragma unroll
                for (int j = 0; j < 4; ++j) {
                    float gv = acc[0][n][m][j];
                    float e = __builtin_amdgcn_exp2f(gv * -1.44269504089f);
                    h[j] = acc[NMAT - 1][n][m][j] * __builtin_amdgcn_rcpf(1.f + e);
                }
                u32x2 v;
                v.x = pack_bf16(h[0], h[1]);
                v.y = pack_bf16(h[2], h[3]);
                *(u32x2*)(a.out + ((size_t)(st16 * 144 + kb)) * 1024 + (g * 16 + r) * 16 + (q & 1) * 8) = v;
            }
    } else {
#pragma unroll
        for (int n = 0; n < NT; ++n)
#pragma unroll
            for (int m = 0; m < MT; ++m) {
                int st16 = tm * (BM / 16) + wm * MT + m;
                int et = tn * 9 + NT0 + n;
                *(f32x4*)(a.out + ((size_t)((ks * 128 + st16) * 36 + et)) * 1024 + lane * 16) = acc[0][n][m];
            }
    }
}

template <int KIND>
__global__ __launch_bounds__(512, 2) void gemm_k(GArgs a) {
    extern __shared__ __attribute__((aligned(16))) char shm[];
    const int wid = __builtin_amdgcn_readfirstlane(threadIdx.x >> 6);
    const int lane = threadIdx.x & 63;
    if (wid < 4) gemm_body<KIND, 5, false>(a, shm, wid, lane);
    else gemm_body<KIND, 4, true>(a, shm, wid, lane);
}

template <int NPART, bool WRITE_X, bool WRITE_XN>
__global__ __launch_bounds__(384) void reduce_ln_k(const u16* __restrict__ xin, const char* __restrict__ part,
                                                   u16* __restrict__ xout, char* __restrict__ xn) {
    __shared__ float red[2][6][8];
    const int w = threadIdx.x >> 6, lane = threadIdx.x & 63;
    const int stripe = blockIdx.x >> 1, half = blockIdx.x & 1;
    const int q = (lane >> 3) & 3, c8 = lane & 7;
    const int row = stripe * 16 + half * 8 + c8;
    const int lam = q * 16 + half * 8 + c8;
    float v[3][4];
    u32x2 xi[3];
    f32x4 p[3][NPART > 0 ? NPART : 1];
#pragma unroll
    for (int j = 0; j < 3; ++j) {
        int et = 2 * (w * 3 + j) + (lane >> 5);
        xi[j] = *(const u32x2*)(xin + (size_t)row * D_ + et * 16 + q * 4);
#pragma unroll
        for (int k = 0; k < NPART; ++k)
            p[j][k] = *(const f32x4*)(part + ((size_t)((k * 128 + stripe) * 36 + et)) * 1024 + lam * 16);
    }
    float s = 0.f;
#pragma unroll
    for (int j = 0; j < 3; ++j) {
        v[j][0] = bf_lo(xi[j].x); v[j][1] = bf_hi(xi[j].x); v[j][2] = bf_lo(xi[j].y); v[j][3] = bf_hi(xi[j].y);
        if (NPART > 0) {
            f32x4 y = p[j][0];
#pragma unroll
            for (int k = 1; k < NPART; ++k) y += p[j][k];
            u32x2 o;
            o.x = pack_bf16(v[j][0] + y[0], v[j][1] + y[1]);
            o.y = pack_bf16(v[j][2] + y[2], v[j][3] + y[3]);
            if (WRITE_X) {
                int et = 2 * (w * 3 + j) + (lane >> 5);
                *(u32x2*)(xout + (size_t)row * D_ + et * 16 + q * 4) = o;
            }
            v[j][0] = bf_lo(o.x); v[j][1] = bf_hi(o.x); v[j][2] = bf_lo(o.y); v[j][3] = bf_hi(o.y);
        }
        s += (v[j][0] + v[j][1]) + (v[j][2] + v[j][3]);
    }
    if (!WRITE_XN) return;
    s += __shfl_xor(s, 8); s += __shfl_xor(s, 16); s += __shfl_xor(s, 32);
    if (lane < 8) red[0][w][lane] = s;
    __syncthreads();
    float mean = 0.f;
#pragma unroll
    for (int k = 0; k < 6; ++k) mean += red[0][k][c8];
    mean *= (1.f / D_);
    float ss = 0.f;
#pragma unroll
    for (int j = 0; j < 3; ++j)
#pragma unroll
        for (int e = 0; e < 4; ++e) { v[j][e] -= mean; ss += v[j][e] * v[j][e]; }
    ss += __shfl_xor(ss, 8); ss += __shfl_xor(ss, 16); ss += __shfl_xor(ss, 32);
    if (lane < 8) red[1][w][lane] = ss;
    __syncthreads();
    float var = 0.f;
#pragma unroll
    for (int k = 0; k < 6; ++k) var += red[1][k][c8];
    const float rs = __builtin_amdgcn_rsqf(var * (1.f / D_) + 1e-5f);
#pragma unroll
    for (int j = 0; j < 3; ++j) {
        int et = 2 * (w * 3 + j) + (lane >> 5);
        int kb = et >> 1, g = 2 * (et & 1) + (q >> 1);
        u32x2 o;
        o.x = pack_bf16(v[j][0] * rs, v[j][1] * rs);
        o.y = pack_bf16(v[j][2] * rs, v[j][3] * rs);
        *(u32x2*)(xn + ((size_t)(stripe * 18 + kb)) * 1024 + (g * 16 + half * 8 + c8) * 16 + (q & 1) * 8) = o;
    }
}

template <int KIND> static void launch_gemm(const GArgs& a, hipStream_t stream) {
    using C = Cfg<KIND>;
    constexpr int shm = C::ST * (C::BM * C::BK * 2 + 18432);
    hipFuncSetAttribute((const void*)gemm_k<KIND>, hipFuncAttributeMaxDynamicSharedMemorySize, shm);
    gemm_k<KIND><<<256, 512, shm, stream>>>(a);
}

extern "C" void kernel_launch(void* const* d_in, const int* in_sizes, int n_in, void* d_out, int out_size, void* d_ws,
                              size_t ws_size, hipStream_t stream) {
    const u16* x0 = (const u16*)d_in[0];
    const char* Cr = (const char*)d_in[1];
    const char* Ci = (const char*)d_in[2];
    const char* Wr = (const char*)d_in[3];
    const char* Wi = (const char*)d_in[4];
    const char* Wg = (const char*)d_in[6];
    const char* Wu = (const char*)d_in[7];
    const char* Wd = (const char*)d_in[8];
    char* ws = (char*)d_ws;
    const size_t XB = (size_t)S_ * D_ * 2;
    char* xn = ws;
    u16* xa = (u16*)(ws + XB);
    u16* xb = (u16*)(ws + 2 * XB);
    char* ab = ws + 3 * XB;
    char* part = ws + 5 * XB;
    char* h = ws + 13 * XB;

    reduce_ln_k<0, false, true><<<256, 384, 0, stream>>>(x0, nullptr, nullptr, xn);
    const u16* xcur = x0;
    for (int layer = 0; layer < 2; ++layer) {
        launch_gemm<G1>(GArgs{xn, xn, Wr, Wi, ab}, stream);
        launch_gemm<G2>(GArgs{Cr, Ci, ab, ab, part}, stream);
        reduce_ln_k<4, true, true><<<256, 384, 0, stream>>>(xcur, part, xa, xn);
        launch_gemm<G3>(GArgs{xn, xn, Wg, Wu, h}, stream);
        launch_gemm<G4>(GArgs{h, h, Wd, Wd, part}, stream);
        if (layer == 0) {
            reduce_ln_k<4, true, true><<<256, 384, 0, stream>>>(xa, part, xb, xn);
            xcur = xb;
        } else {
            reduce_ln_k<4, true, false><<<256, 384, 0, stream>>>(xa, part, (u16*)d_out, nullptr);
        }
    }
}
```

```cpp
#include <hip/hip_runtime.h>
#include <hip/hip_bf16.h>

typedef unsigned short u16;
typedef __attribute__((ext_vector_type(8))) short bf16x8;
typedef __attribute__((ext_vector_type(4))) short bf16x4;
typedef __attribute__((ext_vector_type(4))) float f32x4;
typedef __attribute__((ext_vector_type(2))) float f32x2;
typedef __attribute__((ext_vector_type(2))) unsigned u32x2;
typedef __attribute__((ext_vector_type(4))) unsigned u32x4;
#define LAS __attribute__((address_space(3)))
#define GAS __attribute__((address_space(1)))
typedef GAS unsigned gu32;

constexpr int S_ = 2048, D_ = 576, F_ = 4608;
constexpr size_t XB = (size_t)S_ * D_ * 2;

enum { G1 = 1, G2 = 2, G3 = 3, G4 = 4 };

template <int KIND> struct Cfg;
template <> struct Cfg<G1> { static constexpr int BM = 64,  MT = 1, BK = 64, NK = 9,  ST = 5, MROW = 0, NMODE = 1, MDEP = 1; };
template <> struct Cfg<G2> { static constexpr int BM = 128, MT = 2, BK = 64, NK = 16, ST = 4, MROW = 1, NMODE = 0, MDEP = 0; };
template <> struct Cfg<G3> { static constexpr int BM = 256, MT = 4, BK = 32, NK = 18, ST = 4, MROW = 0, NMODE = 2, MDEP = 1; };
template <> struct Cfg<G4> { static constexpr int BM = 128, MT = 2, BK = 64, NK = 18, ST = 4, MROW = 0, NMODE = 2, MDEP = 1; };

constexpr int LDS_X = 139264;
constexpr int LDS_STAT = LDS_X + 10240;
constexpr int LDS_TOTAL = LDS_STAT + 256;

constexpr int W_XN = 0, W_AB = 1, W_P2 = 3, W_XN2 = 7, W_H = 8, W_P4 = 16, W_LAYER = 20;
constexpr size_t WS_CNT = 40 * XB;
enum { C_AB = 0, C_P2 = 16, C_XN2 = 32, C_H = 40, C_P4 = 72, C_XN4 = 88, C_LAYER = 128 };

__device__ __forceinline__ unsigned pack_bf16(float lo, float hi) {
    typedef __attribute__((ext_vector_type(2))) __bf16 bf16x2_t;
    f32x2 f = {lo, hi};
    bf16x2_t b = __builtin_convertvector(f, bf16x2_t);
    return __builtin_bit_cast(unsigned, b);
}
__device__ __forceinline__ float bf_lo(unsigned v) { return __uint_as_float(v << 16); }
__device__ __forceinline__ float bf_hi(unsigned v) { return __uint_as_float(v & 0xffff0000u); }

template <bool SC1> __device__ __forceinline__ void glds16(const char* gbase, unsigned voff, unsigned lds) {
    if (SC1) asm volatile("s_mov_b32 m0, %0\n\ts_nop 0\n\tglobal_load_lds_dwordx4 %1, %2 sc1" ::"s"(lds), "v"(voff), "s"(gbase) : "m0");
    else asm volatile("s_mov_b32 m0, %0\n\ts_nop 0\n\tglobal_load_lds_dwordx4 %1, %2" ::"s"(lds), "v"(voff), "s"(gbase) : "m0");
}
template <int N> __device__ __forceinline__ void wait_vm() { asm volatile("s_waitcnt vmcnt(%0)" ::"i"(N) : "memory"); }
__device__ __forceinline__ void wait_lgkm0() {
    __builtin_amdgcn_sched_barrier(0);
    __builtin_amdgcn_s_waitcnt(0xC07F);
    __builtin_amdgcn_sched_barrier(0);
}
__device__ __forceinline__ void bar() {
    __builtin_amdgcn_sched_barrier(0);
    asm volatile("s_barrier" ::: "memory");
    __builtin_amdgcn_sched_barrier(0);
}
__device__ __forceinline__ __amdgpu_buffer_rsrc_t rsrc(const void* p, unsigned bytes) {
    return __builtin_amdgcn_make_buffer_rsrc((void*)p, (short)0, (int)bytes, 0x00020000);
}
__device__ __forceinline__ void st16(__amdgpu_buffer_rsrc_t r, unsigned off, u32x4 v) { __builtin_amdgcn_raw_buffer_store_b128(v, r, off, 0, 16); }
__device__ __forceinline__ void st8(__amdgpu_buffer_rsrc_t r, unsigned off, u32x2 v) { __builtin_amdgcn_raw_buffer_store_b64(v, r, off, 0, 16); }
__device__ __forceinline__ u32x4 ld16(__amdgpu_buffer_rsrc_t r, unsigned off) { return __builtin_amdgcn_raw_buffer_load_b128(r, off, 0, 16); }
__device__ __forceinline__ u32x2 ld8(__amdgpu_buffer_rsrc_t r, unsigned off) { return __builtin_amdgcn_raw_buffer_load_b64(r, off, 0, 16); }

#define STAMP(i)

struct Params {
    const char* x0;
    const char* Cr;
    const char* Ci;
    const char* Wr;
    const char* Wi;
    const char* Wg;
    const char* Wu;
    const char* Wd;
    char* out;
    char* ws;
};

typedef const __attribute__((address_space(4))) Params* ParamsPtr;
__device__ __forceinline__ Params load_params() {
    ParamsPtr kp = (ParamsPtr)__builtin_amdgcn_kernarg_segment_ptr();
    asm volatile("" : "+s"(kp));
    Params p;
    p.x0 = kp->x0; p.Cr = kp->Cr; p.Ci = kp->Ci; p.Wr = kp->Wr; p.Wi = kp->Wi;
    p.Wg = kp->Wg; p.Wu = kp->Wu; p.Wd = kp->Wd; p.out = kp->out; p.ws = kp->ws;
    return p;
}
__device__ __forceinline__ int opaque_lane() {
    int l;
    asm volatile("v_mbcnt_lo_u32_b32 %0, -1, 0\n\tv_mbcnt_hi_u32_b32 %0, -1, %0" : "=v"(l));
    return l;
}
#define WL(p, layer) ((p).ws + (size_t)(layer) * (W_LAYER * XB))
__device__ __forceinline__ void signal(const Params& p, int id, int prod, int lane) {
    if (lane < 4) __hip_atomic_store((gu32*)(p.ws + WS_CNT) + (id * 4 + lane) * 64 + prod, 1u, __ATOMIC_RELAXED, __HIP_MEMORY_SCOPE_AGENT);
}
template <int NPROD> __device__ __forceinline__ void poll(const Params& p, int id, int cons, int lane) {
    gu32* c = (gu32*)(p.ws + WS_CNT) + (id * 4 + (cons & 3)) * 64 + (lane & (NPROD - 1));
    gu32* tmo = (gu32*)(p.ws + WS_CNT) + 1024 * 64;
    for (unsigned spins = 0;; ++spins) {
        unsigned v = __hip_atomic_load(c, __ATOMIC_RELAXED, __HIP_MEMORY_SCOPE_AGENT);
        if (__builtin_amdgcn_ballot_w64(v == 0u) == 0ull) break;
        if (spins > (1u << 20)) { __hip_atomic_store(tmo, 1u, __ATOMIC_RELAXED, __HIP_MEMORY_SCOPE_AGENT); break; }
        if ((spins & 1023u) == 1023u && __builtin_amdgcn_readfirstlane(__hip_atomic_load(tmo, __ATOMIC_RELAXED, __HIP_MEMORY_SCOPE_AGENT))) break;
    }
    asm volatile("" ::: "memory");
}
__device__ __forceinline__ u32x4 merge16(u32x2 a, u32x2 b) {
    auto r0 = __builtin_amdgcn_permlane16_swap(a.x, b.x, false, false);
    auto r1 = __builtin_amdgcn_permlane16_swap(a.y, b.y, false, false);
    return (u32x4){r0[0], r1[0], r0[1], r1[1]};
}
__device__ __forceinline__ u32x2 pack4(f32x4 v) { return (u32x2){pack_bf16(v[0], v[1]), pack_bf16(v[2], v[3])}; }

template <int KIND, bool OPAQUE = true> __device__ __forceinline__ void tile_map(int& tm, int& tn, int& ks) {
    int bid = blockIdx.x;
    if (OPAQUE) asm volatile("" : "+s"(bid));
    const int xcd = bid & 7, loc = bid >> 3;
    ks = 0;
    if (KIND == G1) { tm = xcd * 4 + (loc >> 3); tn = loc & 7; }
    else if (KIND == G3) { tm = (xcd & 1) * 4 + (loc >> 3); tn = (xcd >> 1) * 8 + (loc & 7); }
    else { ks = xcd >> 1; tm = (xcd & 1) * 8 + (loc >> 2); tn = loc & 3; }
}

__device__ __forceinline__ unsigned lds_base() {
    extern __shared__ __attribute__((aligned(16))) char shm_[];
    return (unsigned)(size_t)(LAS char*)shm_;
}
template <int KIND, int MI> struct MLoad {
    const char* g;
    unsigned off[MI];
    unsigned lds;
    static constexpr int STEP = Cfg<KIND>::MROW ? 128 : (Cfg<KIND>::BK / 32) * 1024;
    __device__ __forceinline__ void init(const Params& p, const char* wl, int w4, int lane) {
        using C = Cfg<KIND>;
        constexpr int BM = C::BM, NK = C::NK, KK = C::BK / 32;
        int tm, tn, ks;
        tile_map<KIND>(tm, tn, ks);
        lds = lds_base() + w4 * (MI * 1024);
        if (C::MROW) {
            g = (ks >= 2 ? p.Ci : p.Cr) + (size_t)(tm * BM) * (S_ * 2) + (ks & 1) * (NK * 128);
#pragma unroll
            for (int i = 0; i < MI; ++i) {
                int row = (w4 * MI + i) * 8 + (lane >> 3);
                int c = (lane & 7) ^ (row & 7);
                off[i] = row * (S_ * 2) + c * 16;
            }
        } else {
            constexpr int NKB = (KIND == G4) ? (F_ / 32) : (D_ / 32);
            const char* base = wl + (KIND == G1 ? W_XN : KIND == G3 ? W_XN2 : W_H) * XB;
            g = base + ((size_t)(tm * (BM / 16)) * NKB + ks * (NK * KK)) * 1024;
#pragma unroll
            for (int i = 0; i < MI; ++i) {
                int f = w4 * MI + i;
                int kk = f / (BM / 16), mt = f % (BM / 16);
                off[i] = (mt * NKB + kk) * 1024 + lane * 16;
            }
        }
    }
    __device__ __forceinline__ void issue(int so) {
#pragma unroll
        for (int i = 0; i < MI; ++i) glds16<Cfg<KIND>::MDEP != 0>(g, off[i], lds + so + i * 1024);
        g += STEP;
    }
};
template <int KIND> struct NLoad {
    const char* g;
    unsigned off[5];
    unsigned lds;
    static constexpr int LDW = (KIND == G3) ? F_ : D_;
    static constexpr int STEP = Cfg<KIND>::NMODE == 0 ? (Cfg<KIND>::BK / 32) * 1024 : Cfg<KIND>::NMODE == 1 ? 128 : Cfg<KIND>::BK * LDW * 2;
    __device__ __forceinline__ void init(const Params& p, const char* wl, int w4, int lane) {
        using C = Cfg<KIND>;
        constexpr int NK = C::NK, BK = C::BK, KK = C::BK / 32;
        int tm, tn, ks;
        tile_map<KIND>(tm, tn, ks);
        lds = lds_base() + C::BM * BK * 2 + w4 * 4608;
        if (C::NMODE == 0) {
            g = wl + W_AB * XB + ((size_t)(tn * 9) * 128 + ks * (NK * KK)) * 1024;
#pragma unroll
            for (int i = 0; i < 5; ++i) {
                int o = w4 * 4608 + i * 1024 + (i == 4 ? (lane & 31) : lane) * 16;
                int f = o >> 10, w = o & 1023;
                int kk = f / 9, nt = f % 9;
                off[i] = (nt * 128 + kk) * 1024 + w;
            }
        } else if (C::NMODE == 1) {
            g = (tn >= 4 ? p.Wi : p.Wr) + (size_t)((tn & 3) * 144) * (D_ * 2);
#pragma unroll
            for (int i = 0; i < 5; ++i) {
                int o = w4 * 4608 + i * 1024 + (i == 4 ? (lane & 31) : lane) * 16;
                int row = o >> 7;
                int c = ((o >> 4) & 7) ^ (row & 7);
                off[i] = row * (D_ * 2) + c * 16;
            }
        } else {
            const char* wsel = (KIND == G3) ? (w4 >= 2 ? p.Wu : p.Wg) : p.Wd;
            g = wsel + (size_t)(ks * (NK * BK)) * (LDW * 2) + tn * 288;
#pragma unroll
            for (int i = 0; i < 5; ++i) {
                int o = (KIND == G3 ? (w4 & 1) : w4) * 4608 + i * 1024 + (i == 4 ? (lane & 31) : lane) * 16;
                int slot = o / 288, cb = o - slot * 288;
                int kr = (slot & ~12) | ((slot & 4) << 1) | ((slot & 8) >> 1);
                off[i] = kr * (LDW * 2) + cb;
            }
        }
    }
    __device__ __forceinline__ void issue(int so, int lane) {
#pragma unroll
        for (int i = 0; i < 4; ++i) glds16<Cfg<KIND>::MDEP == 0>(g, off[i], lds + so + i * 1024);
        if (lane < 32) glds16<Cfg<KIND>::MDEP == 0>(g, off[4], lds + so + 4096);
        g += STEP;
    }
};

template <int NMODE> __device__ __forceinline__ bf16x8 read_n(const char* p, int n) {
    if (NMODE == 0) return *(const bf16x8*)(p + n * 1024);
    else if (NMODE == 1) return *(const bf16x8*)(p + n * 2048);
    else {
        bf16x4 lo = __builtin_amdgcn_ds_read_tr16_b64_v4i16((LAS bf16x4*)(p + n * 32));
        bf16x4 hi = __builtin_amdgcn_ds_read_tr16_b64_v4i16((LAS bf16x4*)(p + n * 32 + 2304));
        return __builtin_shufflevector(lo, hi, 0, 1, 2, 3, 4, 5, 6, 7);
    }
}

template <int KIND> struct PcLoader {
    static constexpr int MI = Cfg<KIND>::BM * Cfg<KIND>::BK * 2 / 4096;
    static constexpr int STB = Cfg<KIND>::BM * Cfg<KIND>::BK * 2 + 18432;
    MLoad<KIND, MI> m;
    NLoad<KIND> n;
    __device__ __forceinline__ void prefetch(int layer, int w4) {
        const Params p = load_params();
        const char* wl = WL(p, layer);
        const int lane = opaque_lane();
        if (Cfg<KIND>::MDEP) {
            n.init(p, wl, w4, lane);
#pragma unroll
            for (int s = 0; s < Cfg<KIND>::ST; ++s) n.issue(s * STB, lane);
        } else {
            m.init(p, wl, w4, lane);
#pragma unroll
            for (int s = 0; s < Cfg<KIND>::ST; ++s) m.issue(s * STB);
        }
    }
    __device__ __forceinline__ void run(int layer, int w4) {
        using C = Cfg<KIND>;
        constexpr int NK = C::NK, ST = C::ST, CNT = MI + 5, CD = C::MDEP ? MI : 5;
        const Params p = load_params();
        const char* wl = WL(p, layer);
        const int lane = opaque_lane();
        if (C::MDEP) m.init(p, wl, w4, lane); else n.init(p, wl, w4, lane);
        bar();
#pragma unroll
        for (int s = 0; s < ST; ++s) { if (C::MDEP) m.issue(s * STB); else n.issue(s * STB, lane); }
        wait_vm<CD*(ST - 1)>();
        bar();
        int so = 0;
#pragma unroll
        for (int j = 0; j < ST - 2; ++j) {
            if (j == 0) wait_vm<CD*(ST - 2)>();
            if (j == 1) wait_vm<CD*(ST - 3) + CNT>();
            if (j == 2) wait_vm<CD*(ST - 4 > 0 ? ST - 4 : 0) + 2 * CNT>();
            bar();
            m.issue(so); n.issue(so, lane);
            so += STB;
        }
        for (int kt = ST - 2; kt < NK - ST; ++kt) {
            wait_vm<CNT*(ST - 2)>();
            bar();
            m.issue(so); n.issue(so, lane);
            so = (so + STB == ST * STB) ? 0 : so + STB;
        }
#pragma unroll
        for (int t = 0; t < ST - 1; ++t) {
            if (t == 0) wait_vm<CNT*(ST - 2)>();
            if (t == 1) wait_vm<CNT*(ST - 3 > 0 ? ST - 3 : 0)>();
            if (t == 2) wait_vm<CNT*(ST - 4 > 0 ? ST - 4 : 0)>();
            if (t == 3) wait_vm<CNT*(ST - 5 > 0 ? ST - 5 : 0)>();
            bar();
        }
        bar();
    }
};

template <int KIND>
__device__ __forceinline__ void pc_compute(int layer, char* shm, int w4, bool need_poll, int sb) {
    constexpr int STAMP_ROLE = 0;
    const int cbase = layer * C_LAYER;
    using C = Cfg<KIND>;
    constexpr int BM = C::BM, MT = C::MT, BK = C::BK, NK = C::NK, ST = C::ST;
    constexpr int MB = BM * BK * 2, STB = MB + 18432;
    int tm, tn, ks;
    tile_map<KIND>(tm, tn, ks);
    const int lane = opaque_lane();
    const int r = lane & 15, q = lane >> 4;
    int maddr[2], naddr[2];
#pragma unroll
    for (int kk = 0; kk < 2; ++kk) {
        if (C::MROW) maddr[kk] = (w4 * MT * 16 + r) * 128 + (((kk * 4 + q) ^ (r & 7)) * 16);
        else maddr[kk] = (kk * (BM / 16) + w4 * MT) * 1024 + lane * 16;
        if (C::NMODE == 0) naddr[kk] = MB + (kk * 9) * 1024 + lane * 16;
        else if (C::NMODE == 1) naddr[kk] = MB + r * 128 + (((kk * 4 + q) ^ (r & 7)) * 16);
        else naddr[kk] = MB + (kk * 32 + 16 * (q >> 1) + 4 * (q & 1) + ((lane >> 2) & 3)) * 288 + (lane & 3) * 8;
    }
    f32x4 acc[9][MT];
#pragma unroll
    for (int n = 0; n < 9; ++n)
#pragma unroll
        for (int m = 0; m < MT; ++m) acc[n][m] = (f32x4){0.f, 0.f, 0.f, 0.f};

    bf16x8 xm[MT], xn_[9], ym[MT], yn[9];
    auto load = [&](bf16x8(&fm)[MT], bf16x8(&fn)[9], int so, int kk) {
        const char* sbp = shm + so;
#pragma unroll
        for (int m = 0; m < MT; ++m) fm[m] = *(const bf16x8*)(sbp + maddr[kk] + m * (C::MROW ? 2048 : 1024));
#pragma unroll
        for (int n = 0; n < 9; ++n) fn[n] = read_n<C::NMODE>(sbp + naddr[kk], n);
    };
    auto mma = [&](bf16x8(&fm)[MT], bf16x8(&fn)[9]) {
#pragma unroll
        for (int n = 0; n < 9; ++n)
#pragma unroll
            for (int m = 0; m < MT; ++m) {
                if (KIND == G1) acc[n][m] = __builtin_amdgcn_mfma_f32_16x16x32_bf16(fm[m], fn[n], acc[n][m], 0, 0, 0);
                else acc[n][m] = __builtin_amdgcn_mfma_f32_16x16x32_bf16(fn[n], fm[m], acc[n][m], 0, 0, 0);
            }
    };
    auto interleave = [&]() {
        constexpr int NRD = MT + (C::NMODE == 2 ? 18 : 9);
        constexpr int NMF = 9 * MT;
        constexpr int PER = (NRD + NMF - 1) / NMF;
#pragma unroll
        for (int i = 0; i < NMF; ++i) {
            __builtin_amdgcn_sched_group_barrier(0x008, 1, 0);
            __builtin_amdgcn_sched_group_barrier(0x100, PER, 0);
        }
    };

    if (w4 == 0 && need_poll) {
        const Params p = load_params();
        if (KIND == G1) poll<8>(p, cbase - C_LAYER + C_XN4 + tm, tn, lane);
        if (KIND == G2) poll<16>(p, cbase + C_AB + ks * 4 + tn, tm, lane);
        if (KIND == G4) poll<8>(p, cbase + C_H + (tm >> 1) * 4 + ks, tn, lane);
    }
    STAMP(sb + 0);
    bar();
    bar();
    STAMP(sb + 1);
    load(xm, xn_, 0, 0);
    int so = 0;
    for (int kt = 0; kt < NK - 1; ++kt) {
        load(ym, yn, so, 1);
        mma(xm, xn_);
        interleave();
        wait_lgkm0();
        bar();
        so = (so + STB == ST * STB) ? 0 : so + STB;
        load(xm, xn_, so, 0);
        mma(ym, yn);
        interleave();
        __builtin_amdgcn_sched_barrier(0);
    }
    load(ym, yn, so, 1);
    mma(xm, xn_);
    interleave();
    wait_lgkm0();
    bar();
    mma(ym, yn);
    __builtin_amdgcn_sched_barrier(0);
    STAMP(sb + 2);

    const Params p = load_params();
    char* wl = WL(p, layer);
    tile_map<KIND>(tm, tn, ks);
    const int l2 = opaque_lane();
    const int r2 = l2 & 15, q2 = l2 >> 4;
    if (KIND == G1) {
        const int part = tn >> 2;
        const float sg = part ? -1.f : 1.f;
        __amdgpu_buffer_rsrc_t ro = rsrc(wl + W_AB * XB, 2 * XB);
        const int st16_ = tm * (BM / 16) + w4;
        const int kb = part * 64 + (st16_ >> 1);
        const int g = 2 * (st16_ & 1) + (q2 >> 1);
        const unsigned ob = (((tn & 3) * 9) * 128 + kb) * 1024 + (g * 16 + r2) * 16;
#pragma unroll
        for (int n = 0; n < 8; n += 2)
            st16(ro, ob + (n + (q2 & 1)) * (128 * 1024), merge16(pack4(acc[n][0] * sg), pack4(acc[n + 1][0] * sg)));
        const u32x2 t8 = pack4(acc[8][0] * sg);
        const u32x4 v8 = merge16(t8, t8);
        if (!(q2 & 1)) st16(ro, ob + 8 * (128 * 1024), v8);
    } else {
        __amdgpu_buffer_rsrc_t ro = rsrc(wl + (KIND == G2 ? W_P2 : W_P4) * XB, 4 * XB);
        const unsigned pb = ((ks * 64 + tm * 4 + w4) * 4 + tn) * 9216 + l2 * 16;
#pragma unroll
        for (int m = 0; m < MT; ++m)
#pragma unroll
            for (int pp = 0; pp < 4; ++pp) {
                const u32x2 a = pack4(acc[2 * pp][m]), b = pack4(acc[2 * pp + 1][m]);
                st16(ro, pb + m * 4096 + pp * 1024, (u32x4){a.x, a.y, b.x, b.y});
            }
        const u32x2 a = pack4(acc[8][0]), b = pack4(acc[8][MT - 1]);
        st16(ro, pb + 8192, (u32x4){a.x, a.y, b.x, b.y});
    }
    wait_vm<0>();
    bar();
    if (w4 == 0) {
        if (KIND == G1) signal(p, cbase + C_AB + ((tn >> 2) * 2 + (tm >> 4)) * 4 + (tn & 3), tm & 15, l2);
        if (KIND == G2) signal(p, cbase + C_P2 + tm, ks * 4 + tn, l2);
        if (KIND == G4) signal(p, cbase + C_P4 + tm, ks * 4 + tn, l2);
    }
    STAMP(sb + 3);
}

struct XCols {
    int col[5], lam, row, stripe, half, c8, j;
    bool has4;
    template <bool OPAQUE> __device__ __forceinline__ void init(int tn, int lane) {
        int tm, tnn, ks;
        tile_map<G2, OPAQUE>(tm, tnn, ks);
        j = ks * 4 + tnn;
        stripe = tm * 8 + (j >> 1);
        half = j & 1;
        const int u = lane >> 5, q = (lane >> 3) & 3;
        c8 = lane & 7;
        row = stripe * 16 + half * 8 + c8;
        lam = q * 16 + half * 8 + c8;
        col[0] = tn * 144 + u * 32 + q * 4;        col[1] = col[0] + 16;
        col[2] = tn * 144 + (2 + u) * 32 + q * 4;  col[3] = col[2] + 16;
        col[4] = tn * 144 + 128 + q * 4;
        has4 = (u == 0);
    }
};

__device__ __forceinline__ void x_chunks(const XCols& X, const u32x2 (&o)[5], int lane, u32x4 (&ch)[3], int (&ccol)[3]) {
    const bool odd = (lane >> 3) & 1;
#pragma unroll
    for (int c = 0; c < 3; ++c) {
        const u32x2 A = o[2 * c], B = o[c < 2 ? 2 * c + 1 : 4];
        const u32x2 snd = odd ? A : B;
        u32x2 rcv;
        rcv.x = __builtin_amdgcn_update_dpp(0u, snd.x, 0x128, 0xf, 0xf, false);
        rcv.y = __builtin_amdgcn_update_dpp(0u, snd.y, 0x128, 0xf, 0xf, false);
        ch[c] = odd ? (u32x4){rcv.x, rcv.y, B.x, B.y} : (u32x4){A.x, A.y, rcv.x, rcv.y};
        ccol[c] = odd ? X.col[c < 2 ? 2 * c + 1 : 4] - 4 : X.col[2 * c];
    }
}

template <bool AFTER_G2, bool FROM_X0, bool FINAL>
__device__ __forceinline__ void x_phase(int layer, char* shm, int tn, int sb) {
    constexpr int STAMP_ROLE = 0;
    const int w4 = tn;
    const Params p = load_params();
    const char* part = WL(p, layer) + (AFTER_G2 ? W_P2 : W_P4) * XB;
    char* xn_out = WL(p, layer) + (AFTER_G2 ? W_XN2 : W_LAYER + W_XN) * XB;
    const int lane = opaque_lane();
    XCols X;
    X.init<true>(tn, lane);
    const int panel = X.stripe >> 3;
    const int c_wait = layer * C_LAYER + (AFTER_G2 ? C_P2 : C_P4) + panel;
    const int c_sig = layer * C_LAYER + (AFTER_G2 ? C_XN2 + (panel >> 1) : C_XN4 + (X.stripe >> 2));
    const int prod = AFTER_G2 ? (panel & 1) * 16 + X.j : (X.j & 7);
    const int u = lane >> 5;
    u32x2 xi[5];
    u32x2* xl = (u32x2*)(shm + LDS_X) + tn * 320 + lane;
    if (FROM_X0) {
#pragma unroll
        for (int j = 0; j < 5; ++j) xi[j] = *(const u32x2*)(p.x0 + ((size_t)X.row * D_ + X.col[j]) * 2);
    } else {
#pragma unroll
        for (int j = 0; j < 5; ++j) xi[j] = xl[j * 64];
    }
    if (tn == 0) poll<16>(p, c_wait, X.j, lane);
    STAMP(sb + 0);
    bar();
    __amdgpu_buffer_rsrc_t rp = rsrc(part, 4 * XB);
    u32x4 pa[4], pb[4];
    u32x2 pc[4];
#pragma unroll
    for (int k = 0; k < 4; ++k) {
        unsigned base = ((k * 64 + (X.stripe >> 1)) * 4 + tn) * 9216 + X.lam * 16;
        pa[k] = ld16(rp, base + (X.stripe & 1) * 4096 + u * 1024);
        pb[k] = ld16(rp, base + (X.stripe & 1) * 4096 + (2 + u) * 1024);
        pc[k] = ld8(rp, base + 8192 + (X.stripe & 1) * 8);
    }
    float v[5][4];
#pragma unroll
    for (int j = 0; j < 5; ++j) {
        float y0 = 0.f, y1 = 0.f, y2 = 0.f, y3 = 0.f;
#pragma unroll
        for (int k = 0; k < 4; ++k) {
            unsigned lo = j == 0 ? pa[k].x : j == 1 ? pa[k].z : j == 2 ? pb[k].x : j == 3 ? pb[k].z : pc[k].x;
            unsigned hi = j == 0 ? pa[k].y : j == 1 ? pa[k].w : j == 2 ? pb[k].y : j == 3 ? pb[k].w : pc[k].y;
            y0 += bf_lo(lo); y1 += bf_hi(lo); y2 += bf_lo(hi); y3 += bf_hi(hi);
        }
        u32x2 o;
        o.x = pack_bf16(bf_lo(xi[j].x) + y0, bf_hi(xi[j].x) + y1);
        o.y = pack_bf16(bf_lo(xi[j].y) + y2, bf_hi(xi[j].y) + y3);
        if (FINAL) xi[j] = o;
        else xl[j * 64] = o;
        v[j][0] = bf_lo(o.x); v[j][1] = bf_hi(o.x); v[j][2] = bf_lo(o.y); v[j][3] = bf_hi(o.y);
    }
    STAMP(sb + 1);
    const bool st2 = X.has4 && !((lane >> 3) & 1);
    if (FINAL) {
        u32x4 ch[3];
        int ccol[3];
        x_chunks(X, xi, lane, ch, ccol);
#pragma unroll
        for (int c = 0; c < 3; ++c)
            if (c < 2 || st2) *(u32x4*)(p.out + ((size_t)X.row * D_ + ccol[c]) * 2) = ch[c];
        return;
    }
    float s = 0.f;
#pragma unroll
    for (int j = 0; j < 4; ++j) s += (v[j][0] + v[j][1]) + (v[j][2] + v[j][3]);
    if (X.has4) s += (v[4][0] + v[4][1]) + (v[4][2] + v[4][3]);
    s += __shfl_xor(s, 8); s += __shfl_xor(s, 16); s += __shfl_xor(s, 32);
    const float mw = s * (1.f / 144.f);
    float ss = 0.f;
#pragma unroll
    for (int j = 0; j < 5; ++j) {
        float t = 0.f;
#pragma unroll
        for (int e = 0; e < 4; ++e) { float d = v[j][e] - mw; t += d * d; }
        if (j < 4 || X.has4) ss += t;
    }
    ss += __shfl_xor(ss, 8); ss += __shfl_xor(ss, 16); ss += __shfl_xor(ss, 32);
    f32x2* stat = (f32x2*)(shm + LDS_STAT);
    if (lane < 8) stat[tn * 8 + lane] = (f32x2){mw, ss};
    wait_lgkm0();
    bar();
    const f32x2 s0 = stat[X.c8], s1 = stat[8 + X.c8], s2 = stat[16 + X.c8], s3 = stat[24 + X.c8];
    const float mean = (s0.x + s1.x + s2.x + s3.x) * 0.25f;
    const float d0 = s0.x - mean, d1 = s1.x - mean, d2 = s2.x - mean, d3 = s3.x - mean;
    const float var = ((s0.y + s1.y) + (s2.y + s3.y) + 144.f * ((d0 * d0 + d1 * d1) + (d2 * d2 + d3 * d3))) * (1.f / D_);
    const float rs = __builtin_amdgcn_rsqf(var + 1e-5f);
    __amdgpu_buffer_rsrc_t ro = rsrc(xn_out, XB);
#pragma unroll
    for (int j = 0; j < 5; ++j) {
        xi[j].x = pack_bf16((v[j][0] - mean) * rs, (v[j][1] - mean) * rs);
        xi[j].y = pack_bf16((v[j][2] - mean) * rs, (v[j][3] - mean) * rs);
    }
    u32x4 ch[3];
    int ccol[3];
    x_chunks(X, xi, lane, ch, ccol);
#pragma unroll
    for (int c = 0; c < 3; ++c) {
        const int d = ccol[c];
        if (c < 2 || st2) st16(ro, (X.stripe * 18 + (d >> 5)) * 1024 + (((d >> 3) & 3) * 16 + X.half * 8 + X.c8) * 16, ch[c]);
    }
    wait_vm<0>();
    bar();
    if (tn == 0) signal(p, c_sig, prod, lane);
    STAMP(sb + 2);
}

template <int NT, bool TYPEB, class LD, class HOOK>
__device__ __forceinline__ void g3_phase(int layer, char* shm, int w4, LD& ld, HOOK hook, int sb) {
    constexpr int STAMP_ROLE = TYPEB ? 1 : 0;
    const int cbase = layer * C_LAYER;
    using C = Cfg<G3>;
    constexpr int BM = C::BM, MT = C::MT, NK = C::NK, ST = C::ST;
    constexpr int MB = BM * 64, STB = MB + 18432;
    constexpr int CNT = TYPEB ? 5 : 4;
    constexpr int NT0 = TYPEB ? 5 : 0;
    int tm, tn, ks;
    tile_map<G3>(tm, tn, ks);
    const int lane = opaque_lane();
    auto issue = [&](int so) {
        if constexpr (TYPEB) ld.issue(so, lane); else ld.issue(so);
    };
    const int q = lane >> 4;
    const int maddr = (w4 * MT) * 1024 + lane * 16;
    const int naddr = MB + (16 * (q >> 1) + 4 * (q & 1) + ((lane >> 2) & 3)) * 288 + NT0 * 32 + (lane & 3) * 8;

    f32x4 acc[2][NT][MT];
#pragma unroll
    for (int x = 0; x < 2; ++x)
#pragma unroll
        for (int n = 0; n < NT; ++n)
#pragma unroll
            for (int m = 0; m < MT; ++m) acc[x][n][m] = (f32x4){0.f, 0.f, 0.f, 0.f};
    bf16x8 fm[MT], fn[2][NT];
    auto load = [&](int so) {
        const char* sbp = shm + so;
#pragma unroll
        for (int m = 0; m < MT; ++m) fm[m] = *(const bf16x8*)(sbp + maddr + m * 1024);
#pragma unroll
        for (int x = 0; x < 2; ++x)
#pragma unroll
            for (int n = 0; n < NT; ++n) fn[x][n] = read_n<2>(sbp + naddr + x * 9216, n);
    };
    auto mma = [&]() {
        __builtin_amdgcn_s_setprio(1);
#pragma unroll
        for (int x = 0; x < 2; ++x)
#pragma unroll
            for (int n = 0; n < NT; ++n)
#pragma unroll
                for (int m = 0; m < MT; ++m)
                    acc[x][n][m] = __builtin_amdgcn_mfma_f32_16x16x32_bf16(fn[x][n], fm[m], acc[x][n][m], 0, 0, 0);
        __builtin_amdgcn_s_setprio(0);
    };

    if constexpr (!TYPEB) {
        const Params p = load_params();
        ld.init(p, WL(p, layer), w4, lane);
        if (w4 == 0) poll<32>(p, cbase + C_XN2 + tm, tn, lane);
    }
    STAMP(sb + 0);
    bar();
    if constexpr (!TYPEB) {
#pragma unroll
        for (int s = 0; s < ST - 1; ++s) issue(s * STB);
    }
    wait_vm<TYPEB ? 0 : CNT*(ST - 2)>();
    bar();
    if (TYPEB) bar();
    int rd = 0, wr = (ST - 1) * STB;
    for (int j = 0; j < NK - (ST - 1); ++j) {
        load(rd);
        issue(wr);
        wait_lgkm0();
        if (TYPEB) wait_vm<CNT*(ST - 2)>();
        bar();
        mma();
        if (!TYPEB) wait_vm<CNT*(ST - 2)>();
        bar();
        wr = rd;
        rd = (rd + STB == ST * STB) ? 0 : rd + STB;
    }
#pragma unroll
    for (int t = 0; t < ST - 1; ++t) {
        load(rd);
        wait_lgkm0();
        if (TYPEB) {
            if (t == 0) wait_vm<CNT*(ST - 3)>();
            if (t == 1) wait_vm<0>();
        }
        bar();
        mma();
        if (!TYPEB) {
            if (t == 0) wait_vm<CNT*(ST - 3)>();
            if (t == 1) wait_vm<0>();
        }
        if (!(TYPEB && t == ST - 2)) bar();
        rd = (rd + STB == ST * STB) ? 0 : rd + STB;
    }
    __builtin_amdgcn_sched_barrier(0);
    STAMP(sb + 2);

    const Params p = load_params();
    char* wl = WL(p, layer);
    tile_map<G3>(tm, tn, ks);
    const int l2 = opaque_lane();
    const int r2 = l2 & 15, q2 = l2 >> 4;
    __amdgpu_buffer_rsrc_t ro = rsrc(wl + W_H * XB, 8 * XB);
#pragma unroll
    for (int n = 0; n < NT; ++n) {
        const int f16 = tn * 9 + NT0 + n;
        const unsigned ob = ((tm * (BM / 16) + w4 * MT + (q2 & 1)) * 144 + (f16 >> 1)) * 1024 + ((2 * (f16 & 1) + (q2 >> 1)) * 16 + r2) * 16;
#pragma unroll
        for (int m = 0; m < MT; m += 2) {
            u32x2 t[2];
#pragma unroll
            for (int mm = 0; mm < 2; ++mm) {
                f32x4 h;
#pragma unroll
                for (int j = 0; j < 4; ++j) {
                    float e = __builtin_amdgcn_exp2f(acc[0][n][m + mm][j] * -1.44269504089f);
                    h[j] = acc[1][n][m + mm][j] * __builtin_amdgcn_rcpf(1.f + e);
                }
                t[mm] = pack4(h);
            }
            st16(ro, ob + m * (144 * 1024), merge16(t[0], t[1]));
        }
    }
    wait_vm<0>();
    bar();
    if (!TYPEB && w4 == 0) signal(p, cbase + C_H + tm * 4 + (tn >> 3), tn & 7, l2);
    if (TYPEB) hook();
    STAMP(sb + 3);
}

__global__ __launch_bounds__(512, 2) void mega_k(Params p_unused) {
    extern __shared__ __attribute__((aligned(16))) char shm[];
    const int wid = __builtin_amdgcn_readfirstlane(threadIdx.x >> 6);
    const int w4 = wid & 3;
    if (wid < 4) {
        for (int layer = 0; layer < 2; ++layer) {
            const int sb = layer * 32;
            pc_compute<G1>(layer, shm, w4, layer != 0, sb + 0);
            pc_compute<G2>(layer, shm, w4, true, sb + 4);
            if (layer == 0) x_phase<true, true, false>(layer, shm, w4, sb + 8);
            else x_phase<true, false, false>(layer, shm, w4, sb + 8);
            MLoad<G3, 4> ml;
            g3_phase<5, false>(layer, shm, w4, ml, []() {}, sb + 12);
            pc_compute<G4>(layer, shm, w4, true, sb + 16);
            if (layer == 0) x_phase<false, false, false>(layer, shm, w4, sb + 20);
            else x_phase<false, false, true>(layer, shm, w4, sb + 20);
        }
    } else {
        PcLoader<G1> l1;
        l1.prefetch(0, w4);
        for (int layer = 0; layer < 2; ++layer) {
            const int sb = layer * 32;
            l1.run(layer, w4);
            PcLoader<G2> l2;
            bar();
            l2.prefetch(layer, w4);
            l2.run(layer, w4);
            bar();
            NLoad<G3> l3;
            {
                const Params p = load_params();
                const int lane = opaque_lane();
                l3.init(p, WL(p, layer), w4, lane);
#pragma unroll
                for (int s = 0; s < Cfg<G3>::ST - 1; ++s) l3.issue(s * 34816, lane);
            }
            bar(); bar(); bar();
            PcLoader<G4> l4;
            g3_phase<4, true>(layer, shm, w4, l3, [&]() { l4.prefetch(layer, w4); }, sb + 12);
            l4.run(layer, w4);
            bar();
            if (layer == 0) l1.prefetch(1, w4);
            bar();
            if (layer == 0) { bar(); bar(); }
        }
    }
}

__global__ __launch_bounds__(256) void setup_k(Params p) {
    __shared__ f32x2 stat[32];
    const int tn = threadIdx.x >> 6, lane = threadIdx.x & 63;
    ((unsigned*)(p.ws + WS_CNT))[blockIdx.x * 256 + threadIdx.x] = 0u;
    if (blockIdx.x == 0 && threadIdx.x == 0) ((unsigned*)(p.ws + WS_CNT))[1024 * 64] = 0u;
    XCols X;
    X.init<false>(tn, lane);
    float v[5][4];
#pragma unroll
    for (int j = 0; j < 5; ++j) {
        u32x2 o = *(const u32x2*)(p.x0 + ((size_t)X.row * D_ + X.col[j]) * 2);
        v[j][0] = bf_lo(o.x); v[j][1] = bf_hi(o.x); v[j][2] = bf_lo(o.y); v[j][3] = bf_hi(o.y);
    }
    float s = 0.f;
#pragma unroll
    for (int j = 0; j < 4; ++j) s += (v[j][0] + v[j][1]) + (v[j][2] + v[j][3]);
    if (X.has4) s += (v[4][0] + v[4][1]) + (v[4][2] + v[4][3]);
    s += __shfl_xor(s, 8); s += __shfl_xor(s, 16); s += __shfl_xor(s, 32);
    const float mw = s * (1.f / 144.f);
    float ss = 0.f;
#pragma unroll
    for (int j = 0; j < 5; ++j) {
        float t = 0.f;
#pragma unroll
        for (int e = 0; e < 4; ++e) { float d = v[j][e] - mw; t += d * d; }
        if (j < 4 || X.has4) ss += t;
    }
    ss += __shfl_xor(ss, 8); ss += __shfl_xor(ss, 16); ss += __shfl_xor(ss, 32);
    if (lane < 8) stat[tn * 8 + lane] = (f32x2){mw, ss};
    __syncthreads();
    const f32x2 s0 = stat[X.c8], s1 = stat[8 + X.c8], s2 = stat[16 + X.c8], s3 = stat[24 + X.c8];
    const float mean = (s0.x + s1.x + s2.x + s3.x) * 0.25f;
    const float d0 = s0.x - mean, d1 = s1.x - mean, d2 = s2.x - mean, d3 = s3.x - mean;
    const float var = ((s0.y + s1.y) + (s2.y + s3.y) + 144.f * ((d0 * d0 + d1 * d1) + (d2 * d2 + d3 * d3))) * (1.f / D_);
    const float rs = __builtin_amdgcn_rsqf(var + 1e-5f);
    char* xn = p.ws + W_XN * XB;
#pragma unroll
    for (int j = 0; j < 5; ++j) {
        int d = X.col[j];
        u32x2 o;
        o.x = pack_bf16((v[j][0] - mean) * rs, (v[j][1] - mean) * rs);
        o.y = pack_bf16((v[j][2] - mean) * rs, (v[j][3] - mean) * rs);
        if (j < 4 || X.has4)
            *(u32x2*)(xn + (X.stripe * 18 + (d >> 5)) * 1024 + (((d >> 3) & 3) * 16 + X.half * 8 + X.c8) * 16 + (d & 4) * 2) = o;
    }
}

extern "C" void kernel_launch(void* const* d_in, const int* in_sizes, int n_in, void* d_out, int out_size, void* d_ws,
                              size_t ws_size, hipStream_t stream) {
    Params p;
    p.x0 = (const char*)d_in[0];
    p.Cr = (const char*)d_in[1];
    p.Ci = (const char*)d_in[2];
    p.Wr = (const char*)d_in[3];
    p.Wi = (const char*)d_in[4];
    p.Wg = (const char*)d_in[6];
    p.Wu = (const char*)d_in[7];
    p.Wd = (const char*)d_in[8];
    p.out = (char*)d_out;
    p.ws = (char*)d_ws;
    (void)hipFuncSetAttribute((const void*)mega_k, hipFuncAttributeMaxDynamicSharedMemorySize, LDS_TOTAL);
    setup_k<<<256, 256, 0, stream>>>(p);
    mega_k<<<256, 512, LDS_TOTAL, stream>>>(p);
}
```

```cpp
#include <hip/hip_runtime.h>
#pragma clang diagnostic ignored "-Winline-asm"
#include <hip/hip_bf16.h>
#include <utility>

typedef __attribute__((ext_vector_type(8))) short bf16x8;
typedef __attribute__((ext_vector_type(4))) short bf16x4;
typedef __attribute__((ext_vector_type(4))) float f32x4;
typedef __attribute__((ext_vector_type(2))) float f32x2;
typedef __attribute__((ext_vector_type(2))) unsigned u32x2;
typedef __attribute__((ext_vector_type(4))) unsigned u32x4;
#define LAS __attribute__((address_space(3)))
#define GAS __attribute__((address_space(1)))
typedef GAS unsigned gu32;

constexpr int S_ = 2048, D_ = 576, F_ = 4608;
constexpr size_t XB = (size_t)S_ * D_ * 2;

enum { G1 = 1, G2 = 2, G3 = 3, G4 = 4 };

template <class F, int... I> __device__ __forceinline__ void static_for_impl(F f, std::integer_sequence<int, I...>) { (f(std::integral_constant<int, I>{}), ...); }
template <int N, class F> __device__ __forceinline__ void static_for(F f) { static_for_impl(f, std::make_integer_sequence<int, N>{}); }

template <int KIND> struct Cfg;
template <> struct Cfg<G1> { static constexpr int BM = 64,  MT = 1, BK = 64, NK = 9,  ST = 4, MROW = 0, NMODE = 1, MDEP = 1; };
template <> struct Cfg<G2> { static constexpr int BM = 128, MT = 2, BK = 64, NK = 16, ST = 4, MROW = 1, NMODE = 0, MDEP = 0; };
template <> struct Cfg<G3> { static constexpr int BM = 256, MT = 4, BK = 32, NK = 18, ST = 4, MROW = 0, NMODE = 2, MDEP = 1; };
template <> struct Cfg<G4> { static constexpr int BM = 128, MT = 2, BK = 64, NK = 18, ST = 4, MROW = 0, NMODE = 2, MDEP = 1; };

constexpr int G1_NOFF = 73728;
constexpr int LDS_X = 147456;
constexpr int LDS_STAT = LDS_X + 10240;
constexpr int LDS_SINK = LDS_STAT + 256;
constexpr int LDS_TOTAL = LDS_SINK + 256;

constexpr int W_XN = 0, W_AB = 1, W_P2 = 3, W_XN2 = 7, W_H = 8, W_P4 = 16, W_LAYER = 20;
constexpr size_t WS_CNT = 40 * XB;
enum { C_AB = 0, C_P2 = 16, C_XN2 = 32, C_H = 40, C_P4 = 72, C_XN4 = 88, C_LAYER = 128 };

__device__ __forceinline__ unsigned pack_bf16(float lo, float hi) {
    typedef __attribute__((ext_vector_type(2))) __bf16 bf16x2_t;
    f32x2 f = {lo, hi};
    bf16x2_t b = __builtin_convertvector(f, bf16x2_t);
    return __builtin_bit_cast(unsigned, b);
}
__device__ __forceinline__ float bf_lo(unsigned v) { return __uint_as_float(v << 16); }
__device__ __forceinline__ float bf_hi(unsigned v) { return __uint_as_float(v & 0xffff0000u); }

template <int CTRL> __device__ __forceinline__ float dpp_add(float v) {
    return v + __builtin_bit_cast(float, __builtin_amdgcn_update_dpp(0u, __builtin_bit_cast(unsigned, v), CTRL, 0xf, 0xf, false));
}
__device__ __forceinline__ float sum_lanes_012(float v) {
    v = dpp_add<0xB1>(v); v = dpp_add<0x4E>(v); return dpp_add<0x141>(v);
}
__device__ __forceinline__ float sum_lanes_345(float v) {
    v = dpp_add<0x128>(v);
    unsigned u = __builtin_bit_cast(unsigned, v), u2 = u;
    asm("" : "+v"(u2));
    auto a = __builtin_amdgcn_permlane16_swap(u, u2, false, false);
    v = __uint_as_float(a[0]) + __uint_as_float(a[1]);
    u = __builtin_bit_cast(unsigned, v); u2 = u;
    asm("" : "+v"(u2));
    auto b = __builtin_amdgcn_permlane32_swap(u, u2, false, false);
    return __uint_as_float(b[0]) + __uint_as_float(b[1]);
}
#define OPAQUE_S(x) asm volatile("" : "+s"(x))

template <bool SC1> __device__ __forceinline__ void glds16(const char* gbase, unsigned voff, unsigned lds) {
    if (SC1) asm volatile("s_mov_b32 m0, %0\n\ts_nop 0\n\tglobal_load_lds_dwordx4 %1, %2 sc1" ::"s"(lds), "v"(voff), "s"(gbase) : "m0");
    else asm volatile("s_mov_b32 m0, %0\n\ts_nop 0\n\tglobal_load_lds_dwordx4 %1, %2" ::"s"(lds), "v"(voff), "s"(gbase) : "m0");
}
template <int N> __device__ __forceinline__ void wait_vm() { asm volatile("s_waitcnt vmcnt(%0)" ::"i"(N) : "memory"); }
__device__ __forceinline__ void wait_lgkm0() {
    __builtin_amdgcn_sched_barrier(0);
    __builtin_amdgcn_s_waitcnt(0xC07F);
    __builtin_amdgcn_sched_barrier(0);
}
__device__ __forceinline__ void bar() {
    __builtin_amdgcn_sched_barrier(0);
    asm volatile("s_barrier" ::: "memory");
    __builtin_amdgcn_sched_barrier(0);
}
__device__ __forceinline__ __amdgpu_buffer_rsrc_t rsrc(const void* p, unsigned bytes) {
    return __builtin_amdgcn_make_buffer_rsrc((void*)p, (short)0, (int)bytes, 0x00020000);
}
__device__ __forceinline__ void st16(__amdgpu_buffer_rsrc_t r, unsigned off, u32x4 v) { __builtin_amdgcn_raw_buffer_store_b128(v, r, off, 0, 16); }
__device__ __forceinline__ u32x4 ld16(__amdgpu_buffer_rsrc_t r, unsigned off, unsigned soff) { return __builtin_amdgcn_raw_buffer_load_b128(r, off, soff, 16); }
__device__ __forceinline__ u32x2 ld8(__amdgpu_buffer_rsrc_t r, unsigned off, unsigned soff) { return __builtin_amdgcn_raw_buffer_load_b64(r, off, soff, 16); }


struct Params {
    const char* x0;
    const char* Cr;
    const char* Ci;
    const char* Wr;
    const char* Wi;
    const char* Wg;
    const char* Wu;
    const char* Wd;
    char* out;
    char* ws;
};

typedef const __attribute__((address_space(4))) Params* ParamsPtr;
__device__ __forceinline__ Params load_params() {
    ParamsPtr kp = (ParamsPtr)__builtin_amdgcn_kernarg_segment_ptr();
    asm volatile("" : "+s"(kp));
    Params p;
    p.x0 = kp->x0; p.Cr = kp->Cr; p.Ci = kp->Ci; p.Wr = kp->Wr; p.Wi = kp->Wi;
    p.Wg = kp->Wg; p.Wu = kp->Wu; p.Wd = kp->Wd; p.out = kp->out; p.ws = kp->ws;
    return p;
}
__device__ __forceinline__ int opaque_lane() {
    int l;
    asm volatile("v_mbcnt_lo_u32_b32 %0, -1, 0\n\tv_mbcnt_hi_u32_b32 %0, -1, %0" : "=v"(l));
    return l;
}
#define WL(p, layer) ((p).ws + (size_t)(layer) * (W_LAYER * XB))
constexpr int F_TMO = 1024 * 64, F_EPOCH = F_TMO + 64;
__device__ __forceinline__ void signal(const Params& p, unsigned epoch, int id, int prod, int lane) {
    if (lane < 4) __hip_atomic_store((gu32*)(p.ws + WS_CNT) + ((id * 4 + lane) * 64 + prod), epoch, __ATOMIC_RELAXED, __HIP_MEMORY_SCOPE_AGENT);
}
template <int NPROD> __device__ __forceinline__ void poll(const Params& p, unsigned epoch, int id, int cons, int lane) {
    gu32* c = (gu32*)(p.ws + WS_CNT) + ((id * 4 + (cons & 3)) * 64 + (lane & (NPROD - 1)));
    gu32* tmo = (gu32*)(p.ws + WS_CNT) + F_TMO;
    for (unsigned spins = 0;; ++spins) {
        unsigned v = __hip_atomic_load(c, __ATOMIC_RELAXED, __HIP_MEMORY_SCOPE_AGENT);
        if (__builtin_amdgcn_ballot_w64(v != epoch) == 0ull) break;
        if (spins > (1u << 20)) { __hip_atomic_store(tmo, epoch, __ATOMIC_RELAXED, __HIP_MEMORY_SCOPE_AGENT); break; }
        if ((spins & 1023u) == 1023u && __builtin_amdgcn_readfirstlane(__hip_atomic_load(tmo, __ATOMIC_RELAXED, __HIP_MEMORY_SCOPE_AGENT)) == epoch) break;
    }
    asm volatile("" ::: "memory");
}
__device__ __forceinline__ u32x4 merge16(u32x2 a, u32x2 b) {
    auto r0 = __builtin_amdgcn_permlane16_swap(a.x, b.x, false, false);
    auto r1 = __builtin_amdgcn_permlane16_swap(a.y, b.y, false, false);
    return (u32x4){r0[0], r1[0], r0[1], r1[1]};
}
__device__ __forceinline__ u32x2 pack4(f32x4 v) { return (u32x2){pack_bf16(v[0], v[1]), pack_bf16(v[2], v[3])}; }

template <int KIND, bool OPAQUE = true> __device__ __forceinline__ void tile_map(int& tm, int& tn, int& ks) {
    int bid = blockIdx.x;
    if (OPAQUE) asm volatile("" : "+s"(bid));
    const int xcd = bid & 7, loc = bid >> 3;
    ks = 0;
    if (KIND == G1) { tm = xcd * 4 + (loc >> 3); tn = loc & 7; }
    else if (KIND == G3) { tm = (xcd & 1) * 4 + (loc >> 3); tn = (xcd >> 1) * 8 + (loc & 7); }
    else { ks = xcd >> 1; tm = (xcd & 1) * 8 + (loc >> 2); tn = loc & 3; }
}

__device__ __forceinline__ unsigned lds_base() {
    extern __shared__ __attribute__((aligned(16))) char shm_[];
    return (unsigned)(size_t)(LAS char*)shm_;
}
__device__ __forceinline__ void touch(const char* gbase, unsigned voff) {
    asm volatile("s_mov_b32 m0, %0\n\ts_nop 0\n\tglobal_load_lds_dword %1, %2" ::"s"(lds_base() + LDS_SINK), "v"(voff), "s"(gbase) : "m0");
}
__device__ __forceinline__ void ln_input(char* shm, int wid) {
    const Params p = load_params();
    const int w4 = wid & 3;
    int tm, tn, ks;
    tile_map<G1>(tm, tn, ks);
    const int lane = opaque_lane();
    const int row = wid * 8 + (lane >> 3), c0 = lane & 7;
    const char* src = p.x0 + (size_t)(tm * 64 + row) * (D_ * 2) + c0 * 16;
    u32x4 v[9];
#pragma unroll
    for (int i = 0; i < 9; ++i) v[i] = *(const u32x4*)(src + i * 128);
    float s = 0.f;
#pragma unroll
    for (int i = 0; i < 9; ++i)
#pragma unroll
        for (int e = 0; e < 4; ++e) s += bf_lo(v[i][e]) + bf_hi(v[i][e]);
    s = sum_lanes_012(s);
    const float mean = s * (1.f / D_);
    float ss = 0.f;
#pragma unroll
    for (int i = 0; i < 9; ++i)
#pragma unroll
        for (int e = 0; e < 4; ++e) { const float a = bf_lo(v[i][e]) - mean, b = bf_hi(v[i][e]) - mean; ss += a * a + b * b; }
    ss = sum_lanes_012(ss);
    const float rs = __builtin_amdgcn_rsqf(ss * (1.f / D_) + 1e-5f);
#pragma unroll
    for (int i = 0; i < 9; ++i) {
        u32x4 o;
#pragma unroll
        for (int e = 0; e < 4; ++e) o[e] = pack_bf16((bf_lo(v[i][e]) - mean) * rs, (bf_hi(v[i][e]) - mean) * rs);
        const int c = i * 8 + c0;
        *(u32x4*)(shm + ((c >> 2) * 4 + (row >> 4)) * 1024 + ((c & 3) * 16 + (row & 15)) * 16) = o;
    }
    if (wid < 4) {
        constexpr int NW_ = 41472 / 128, NWI = (NW_ + 255) / 256;
        const int loc = blockIdx.x >> 3;
#pragma unroll
        for (int i = 0; i < NWI; ++i) {
            int t = (i * 4 + w4) * 64 + lane;
            t = t < NW_ ? t : NW_ - 1;
            touch(loc >= 16 ? p.Wi : p.Wr, (loc & 15) * 41472 + t * 128);
        }
    }
    wait_lgkm0();
}
template <int KIND, int MI> struct MLoad {
    const char* g;
    unsigned off[MI];
    unsigned lds;
    static constexpr int STEP = Cfg<KIND>::MROW ? 128 : (Cfg<KIND>::BK / 32) * 1024;
    __device__ __forceinline__ void init(const Params& p, const char* wl, int w4, int lane) {
        using C = Cfg<KIND>;
        constexpr int BM = C::BM, NK = C::NK, KK = C::BK / 32;
        int tm, tn, ks;
        tile_map<KIND>(tm, tn, ks);
        lds = lds_base() + w4 * (MI * 1024);
        if (C::MROW) {
            g = (ks >= 2 ? p.Ci : p.Cr) + (size_t)(tm * BM) * (S_ * 2) + (ks & 1) * (NK * 128);
#pragma unroll
            for (int i = 0; i < MI; ++i) {
                int row = (w4 * MI + i) * 8 + (lane >> 3);
                int c = (lane & 7) ^ (row & 7);
                off[i] = row * (S_ * 2) + c * 16;
            }
        } else {
            constexpr int NKB = (KIND == G4) ? (F_ / 32) : (D_ / 32);
            const char* base = wl + (KIND == G1 ? W_XN : KIND == G3 ? W_XN2 : W_H) * XB;
            g = base + ((size_t)(tm * (BM / 16)) * NKB + ks * (NK * KK)) * 1024;
#pragma unroll
            for (int i = 0; i < MI; ++i) {
                int f = w4 * MI + i;
                int kk = f / (BM / 16), mt = f % (BM / 16);
                off[i] = (mt * NKB + kk) * 1024 + lane * 16;
            }
        }
    }
    __device__ __forceinline__ void issue(int so) {
#pragma unroll
        for (int i = 0; i < MI; ++i) {
            glds16<Cfg<KIND>::MDEP != 0>(g, off[i], lds + so + i * 1024);
        }
        g += STEP;
    }
};
template <int KIND> struct NLoad {
    const char* g;
    unsigned off[5];
    unsigned lds;
    static constexpr int LDW = (KIND == G3) ? F_ : D_;
    static constexpr int STEP = Cfg<KIND>::NMODE == 0 ? (Cfg<KIND>::BK / 32) * 1024 : Cfg<KIND>::NMODE == 1 ? 128 : Cfg<KIND>::BK * LDW * 2;
    __device__ __forceinline__ void init(const Params& p, const char* wl, int w4, int lane) {
        using C = Cfg<KIND>;
        constexpr int NK = C::NK, BK = C::BK, KK = C::BK / 32;
        int tm, tn, ks;
        tile_map<KIND>(tm, tn, ks);
        lds = lds_base() + (KIND == G1 ? G1_NOFF : C::BM * BK * 2) + w4 * 4608;
        if (C::NMODE == 0) {
            g = wl + W_AB * XB + ((size_t)(tn * 9) * 128 + ks * (NK * KK)) * 1024;
#pragma unroll
            for (int i = 0; i < 5; ++i) {
                int o = w4 * 4608 + i * 1024 + (i == 4 ? (lane & 31) : lane) * 16;
                int f = o >> 10, w = o & 1023;
                int kk = f / 9, nt = f % 9;
                off[i] = (nt * 128 + kk) * 1024 + w;
            }
        } else if (C::NMODE == 1) {
            g = (tn >= 4 ? p.Wi : p.Wr) + (size_t)((tn & 3) * 144) * (D_ * 2);
#pragma unroll
            for (int i = 0; i < 5; ++i) {
                int o = w4 * 4608 + i * 1024 + (i == 4 ? (lane & 31) : lane) * 16;
                int row = o >> 7;
                int c = ((o >> 4) & 7) ^ (row & 7);
                off[i] = row * (D_ * 2) + c * 16;
            }
        } else {
            const char* wsel = (KIND == G3) ? (w4 >= 2 ? p.Wu : p.Wg) : p.Wd;
            g = wsel + (size_t)(ks * (NK * BK)) * (LDW * 2) + tn * 288;
#pragma unroll
            for (int i = 0; i < 5; ++i) {
                int o = (KIND == G3 ? (w4 & 1) : w4) * 4608 + i * 1024 + (i == 4 ? (lane & 31) : lane) * 16;
                int slot = o / 288, cb = o - slot * 288;
                int kr = (slot & ~12) | ((slot & 4) << 1) | ((slot & 8) >> 1);
                off[i] = kr * (LDW * 2) + cb;
            }
        }
    }
    __device__ __forceinline__ void issue(int so, int lane) {
#pragma unroll
        for (int i = 0; i < 4; ++i) {
            glds16<Cfg<KIND>::MDEP == 0>(g, off[i], lds + so + i * 1024);
        }
        if (lane < 32) glds16<Cfg<KIND>::MDEP == 0>(g, off[4], lds + so + 4096);
        g += STEP;
    }
};

__device__ __forceinline__ bf16x8 lds16(unsigned a) { return *(const LAS bf16x8*)(size_t)a; }
template <int NMODE> __device__ __forceinline__ bf16x8 read_n(unsigned a, int n) {
    if (NMODE == 0) return lds16(a + n * 1024);
    else if (NMODE == 1) return lds16(a + n * 2048);
    else {
        bf16x4 lo = __builtin_amdgcn_ds_read_tr16_b64_v4i16((LAS bf16x4*)(size_t)(a + n * 32));
        bf16x4 hi = __builtin_amdgcn_ds_read_tr16_b64_v4i16((LAS bf16x4*)(size_t)(a + n * 32 + 2304));
        return __builtin_shufflevector(lo, hi, 0, 1, 2, 3, 4, 5, 6, 7);
    }
}

template <int KIND> struct PcLoader {
    static constexpr int MI = Cfg<KIND>::BM * Cfg<KIND>::BK * 2 / 4096;
    static constexpr int STB = Cfg<KIND>::BM * Cfg<KIND>::BK * 2 + 18432;
    MLoad<KIND, MI> m;
    NLoad<KIND> n;
    static constexpr int CNT = MI + 5, CD = Cfg<KIND>::MDEP ? MI : 5;
    static constexpr int PF = Cfg<KIND>::ST;
    static constexpr int allowed(int j, int L) { int c = 0; for (int s = j + 1; s <= L; ++s) c += (s < PF) ? CD : CNT; return c; }
    __device__ __forceinline__ void prefetch(int layer, int w4) {
        OPAQUE_S(w4);
        const Params p = load_params();
        const char* wl = WL(p, layer);
        const int lane = opaque_lane();
        if (Cfg<KIND>::MDEP) {
            n.init(p, wl, w4, lane);
#pragma unroll
            for (int s = 0; s < PF; ++s) n.issue(s * STB, lane);
        } else {
            m.init(p, wl, w4, lane);
#pragma unroll
            for (int s = 0; s < PF; ++s) m.issue(s * STB);
        }
    }
    __device__ __forceinline__ void run(int layer, int w4) {
        using C = Cfg<KIND>;
        constexpr int NK = C::NK, ST = C::ST;
        OPAQUE_S(w4);
        const Params p = load_params();
        const char* wl = WL(p, layer);
        const int lane = opaque_lane();
        if (C::MDEP) m.init(p, wl, w4, lane); else n.init(p, wl, w4, lane);
        bar();
#pragma unroll
        for (int s = 0; s < ST; ++s) {
            if (C::MDEP || s >= PF) m.issue(s * STB);
            if (!C::MDEP || s >= PF) n.issue(s * STB, lane);
        }
        wait_vm<allowed(0, ST - 1)>();
        bar();
        int so = 0;
        static_for<ST - 2>([&](auto J) {
            wait_vm<allowed(J.value + 1, J.value + ST - 1)>();
            bar();
            m.issue(so); n.issue(so, lane);
            so += STB;
        });
        for (int kt = ST - 2; kt < NK - ST; ++kt) {
            wait_vm<CNT*(ST - 2)>();
            bar();
            m.issue(so); n.issue(so, lane);
            so = (so + STB == ST * STB) ? 0 : so + STB;
        }
        static_for<ST - 1>([&](auto T) {
            wait_vm<CNT*(ST - 2 - T.value)>();
            bar();
        });
        bar();
    }
};

struct G1Loader {
    NLoad<G1> n;
    static constexpr int allowed(int j) {
        return (j == 0) ? 14 : (j + 1 <= 3) ? 10 : 5 * ((j + 3 < 8 ? j + 3 : 8) - (j + 1));
    }
    template <int NS> __device__ __forceinline__ void prefetch(int w4) {
        OPAQUE_S(w4);
        const Params p = load_params();
        const int lane = opaque_lane();
        n.init(p, p.ws, w4, lane);
#pragma unroll
        for (int s = 0; s < NS; ++s) n.issue(s * 18432, lane);
    }
    __device__ __forceinline__ void run(int layer, char* shm, int w4) {
        MLoad<G1, 2> m;
        OPAQUE_S(w4);
        if (layer == 0) {
            const int lane = opaque_lane();
            n.issue(2 * 18432, lane);
            n.issue(3 * 18432, lane);
            wait_vm<10>();
            bar();
        } else {
            const Params p = load_params();
            m.init(p, WL(p, layer), w4, opaque_lane());
            bar();
#pragma unroll
            for (int s = 0; s < 9; ++s) m.issue(s * 8192);
        }
        const int lane = opaque_lane();
        wait_vm<16>();
        bar();
        static_for<8>([&](auto J) {
            wait_vm<allowed(J.value)>();
            bar();
            if (J.value + 4 <= 8) n.issue((J.value & 3) * 18432, lane);
        });
        bar();
    }
};

template <int KIND>
__device__ __forceinline__ void pc_compute(int layer, char* shm, int w4, unsigned epoch) {
    OPAQUE_S(w4); OPAQUE_S(epoch);
    const int cbase = layer * C_LAYER;
    using C = Cfg<KIND>;
    constexpr int BM = C::BM, MT = C::MT, BK = C::BK, NK = C::NK, ST = C::ST;
    constexpr int MB = BM * BK * 2, STB = MB + 18432;
    int tm, tn, ks;
    tile_map<KIND>(tm, tn, ks);
    const int lane = opaque_lane();
    const int r = lane & 15, q = lane >> 4;
    int maddr[2], naddr[2];
#pragma unroll
    for (int kk = 0; kk < 2; ++kk) {
        if (C::MROW) maddr[kk] = (w4 * MT * 16 + r) * 128 + (((kk * 4 + q) ^ (r & 7)) * 16);
        else maddr[kk] = (kk * (BM / 16) + w4 * MT) * 1024 + lane * 16;
        if (C::NMODE == 0) naddr[kk] = MB + (kk * 9) * 1024 + lane * 16;
        else if (C::NMODE == 1) naddr[kk] = G1_NOFF + r * 128 + (((kk * 4 + q) ^ (r & 7)) * 16);
        else naddr[kk] = MB + (kk * 32 + 16 * (q >> 1) + 4 * (q & 1) + ((lane >> 2) & 3)) * 288 + (lane & 3) * 8;
        maddr[kk] += lds_base();
        naddr[kk] += lds_base();
    }
    f32x4 acc[9][MT];
#pragma unroll
    for (int n = 0; n < 9; ++n)
#pragma unroll
        for (int m = 0; m < MT; ++m) acc[n][m] = (f32x4){0.f, 0.f, 0.f, 0.f};

    bf16x8 xm[MT], xn_[9], ym[MT], yn[9];
    auto load = [&](bf16x8(&fm)[MT], bf16x8(&fn)[9], int am, int an) {
#pragma unroll
        for (int m = 0; m < MT; ++m) fm[m] = lds16(am + m * (C::MROW ? 2048 : 1024));
#pragma unroll
        for (int n = 0; n < 9; ++n) fn[n] = read_n<C::NMODE>(an, n);
    };
    auto mma = [&](bf16x8(&fm)[MT], bf16x8(&fn)[9]) {
#pragma unroll
        for (int n = 0; n < 9; ++n)
#pragma unroll
            for (int m = 0; m < MT; ++m) {
                if (KIND == G1) acc[n][m] = __builtin_amdgcn_mfma_f32_16x16x32_bf16(fm[m], fn[n], acc[n][m], 0, 0, 0);
                else acc[n][m] = __builtin_amdgcn_mfma_f32_16x16x32_bf16(fn[n], fm[m], acc[n][m], 0, 0, 0);
            }
    };
    auto interleave = [&](auto DBL_) {
        constexpr int NRD = MT + (C::NMODE == 2 ? 18 : 9);
        constexpr int NMF = 9 * MT;
        constexpr int DBL = (NRD > NMF) ? (DBL_.value > NRD - NMF ? DBL_.value : NRD - NMF) : 0;
#pragma unroll
        for (int i = 0; i < NMF; ++i) {
            __builtin_amdgcn_sched_group_barrier(0x008, 1, 0);
            if (i < DBL) __builtin_amdgcn_sched_group_barrier(0x100, 2, 0);
            else __builtin_amdgcn_sched_group_barrier(0x100, 1, 0);
        }
    };
    constexpr std::integral_constant<int, 10> dY{};
    constexpr std::integral_constant<int, 10> dX{};

    const char *tw0 = nullptr, *tw1 = nullptr;
    unsigned tmask = layer == 0 ? ((0x111u << w4) & (KIND == G1 ? 0xffu : 0x1feu)) : 0u;
    const unsigned tof = (KIND == G1) ? (lane >> 2) * (S_ * 2) + (lane & 3) * 128 : lane * 128;
    if (KIND == G1 || KIND == G2) {
        const Params p = load_params();
        const int xcd = blockIdx.x & 7, loc = blockIdx.x >> 3;
        if (KIND == G1) tw0 = ((xcd >> 2) ? p.Ci : p.Cr) + (size_t)((xcd & 1) * 1024 + loc * 32) * (S_ * 2) + ((xcd >> 1) & 1) * 2048;
        else { tw0 = (blockIdx.x >= 128 ? p.Wu : p.Wg) + (blockIdx.x & 127) * 41472; tw1 = p.Wd + blockIdx.x * 20736; }
    }
    if (w4 == 0 && !(KIND == G1 && layer == 0)) {
        const Params p = load_params();
        if (KIND == G1) poll<8>(p, epoch, cbase - C_LAYER + C_XN4 + tm, tn, lane);
        if (KIND == G2) poll<16>(p, epoch, cbase + C_AB + ks * 4 + tn, tm, lane);
        if (KIND == G4) poll<8>(p, epoch, cbase + C_H + (tm >> 1) * 4 + ks, tn, lane);
    }
    bar();
    bar();
    constexpr int NSTB = (KIND == G1) ? 18432 : STB;
    load(xm, xn_, maddr[0], naddr[0]);
    int mo = 0, no = 0;
    int aym = maddr[1], ayn = naddr[1];
    for (int kt = 0; kt < NK - 1; ++kt) {
        load(ym, yn, aym, ayn);
        mma(xm, xn_);
        interleave(dY);
        __builtin_amdgcn_sched_barrier(0);
        no = (no + NSTB == ST * NSTB) ? 0 : no + NSTB;
        mo = (KIND == G1) ? mo + 8192 : no;
        int axm = mo + maddr[0], axn = no + naddr[0];
        aym = mo + maddr[1]; ayn = no + naddr[1];
        asm volatile("" : "+v"(axm), "+v"(axn), "+v"(aym), "+v"(ayn));
        if (KIND == G1 || KIND == G2) {
            __builtin_amdgcn_sched_barrier(0);
            if (tmask & 1u) {
                if (KIND == G1) touch(tw0 + (kt & 1) * (16 * S_ * 2) + (kt >> 1) * 512, tof);
                else touch(kt <= 5 ? tw0 + (kt - 1) * 8192 : tw1 + (kt == 8 ? 20736 - 8192 : (kt - 6) * 8192), tof);
            }
            tmask >>= 1;
        }
        wait_lgkm0();
        bar();
        load(xm, xn_, axm, axn);
        mma(ym, yn);
        interleave(dX);
        __builtin_amdgcn_sched_barrier(0);
    }
    load(ym, yn, aym, ayn);
    mma(xm, xn_);
    interleave(dY);
    wait_lgkm0();
    bar();
    mma(ym, yn);
    __builtin_amdgcn_sched_barrier(0);

    OPAQUE_S(w4); OPAQUE_S(epoch);
    const Params p = load_params();
    char* wl = WL(p, layer);
    tile_map<KIND>(tm, tn, ks);
    const int l2 = opaque_lane();
    const int r2 = l2 & 15, q2 = l2 >> 4;
    if (KIND == G1) {
        const int part = tn >> 2;
        const float sg = part ? -1.f : 1.f;
        __amdgpu_buffer_rsrc_t ro = rsrc(wl + W_AB * XB, 2 * XB);
        const int st16_ = tm * (BM / 16) + w4;
        const int kb = part * 64 + (st16_ >> 1);
        const int g = 2 * (st16_ & 1) + (q2 >> 1);
        const unsigned ob = (((tn & 3) * 9) * 128 + kb) * 1024 + (g * 16 + r2) * 16;
#pragma unroll
        for (int n = 0; n < 8; n += 2)
            st16(ro, ob + (n + (q2 & 1)) * (128 * 1024), merge16(pack4(acc[n][0] * sg), pack4(acc[n + 1][0] * sg)));
        const u32x2 t8 = pack4(acc[8][0] * sg);
        u32x2 t8b = t8;
        asm("" : "+v"(t8b));
        const u32x4 v8 = merge16(t8, t8b);
        if (!(q2 & 1)) st16(ro, ob + 8 * (128 * 1024), v8);
    } else {
        __amdgpu_buffer_rsrc_t ro = rsrc(wl + (KIND == G2 ? W_P2 : W_P4) * XB, 4 * XB);
        const unsigned pb = ((ks * 64 + tm * 4 + w4) * 4 + tn) * 9216 + l2 * 16;
#pragma unroll
        for (int m = 0; m < MT; ++m)
#pragma unroll
            for (int pp = 0; pp < 4; ++pp) {
                const u32x2 a = pack4(acc[2 * pp][m]), b = pack4(acc[2 * pp + 1][m]);
                st16(ro, pb + m * 4096 + pp * 1024, (u32x4){a.x, a.y, b.x, b.y});
            }
        const u32x2 a = pack4(acc[8][0]), b = pack4(acc[8][MT - 1]);
        st16(ro, pb + 8192, (u32x4){a.x, a.y, b.x, b.y});
    }
    wait_vm<0>();
    bar();
    if (w4 == 0) {
        if (KIND == G1) signal(p, epoch, cbase + C_AB + ((tn >> 2) * 2 + (tm >> 4)) * 4 + (tn & 3), tm & 15, l2);
        if (KIND == G2) signal(p, epoch, cbase + C_P2 + tm, ks * 4 + tn, l2);
        if (KIND == G4) signal(p, epoch, cbase + C_P4 + tm, ks * 4 + tn, l2);
    }
}

struct XCols {
    int col[5], lam, row, stripe, half, c8, j;
    bool has4;
    template <bool OPAQUE> __device__ __forceinline__ void init(int tn, int lane) {
        int tm, tnn, ks;
        tile_map<G2, OPAQUE>(tm, tnn, ks);
        j = ks * 4 + tnn;
        stripe = tm * 8 + (j >> 1);
        half = j & 1;
        const int u = lane >> 5, q = (lane >> 3) & 3;
        c8 = lane & 7;
        row = stripe * 16 + half * 8 + c8;
        lam = q * 16 + half * 8 + c8;
        col[0] = tn * 144 + u * 32 + q * 4;        col[1] = col[0] + 16;
        col[2] = tn * 144 + (2 + u) * 32 + q * 4;  col[3] = col[2] + 16;
        col[4] = tn * 144 + 128 + q * 4;
        has4 = (u == 0);
    }
};

__device__ __forceinline__ void x_chunks(const XCols& X, const u32x2 (&o)[5], int lane, u32x4 (&ch)[3], int (&ccol)[3]) {
    const bool odd = (lane >> 3) & 1;
#pragma unroll
    for (int c = 0; c < 3; ++c) {
        const u32x2 A = o[2 * c], B = o[c < 2 ? 2 * c + 1 : 4];
        const u32x2 snd = odd ? A : B;
        u32x2 rcv;
        rcv.x = __builtin_amdgcn_update_dpp(0u, snd.x, 0x128, 0xf, 0xf, false);
        rcv.y = __builtin_amdgcn_update_dpp(0u, snd.y, 0x128, 0xf, 0xf, false);
        ch[c] = odd ? (u32x4){rcv.x, rcv.y, B.x, B.y} : (u32x4){A.x, A.y, rcv.x, rcv.y};
        ccol[c] = odd ? X.col[c < 2 ? 2 * c + 1 : 4] - 4 : X.col[2 * c];
    }
}

template <bool AFTER_G2, bool FROM_X0, bool FINAL>
__device__ __forceinline__ void x_phase(int layer, char* shm, int tn, unsigned epoch) {
    OPAQUE_S(tn); OPAQUE_S(epoch);
    const Params p = load_params();
    const char* part = WL(p, layer) + (AFTER_G2 ? W_P2 : W_P4) * XB;
    char* xn_out = WL(p, layer) + (AFTER_G2 ? W_XN2 : W_LAYER + W_XN) * XB;
    const int lane = opaque_lane();
    XCols X;
    X.init<true>(tn, lane);
    const int panel = X.stripe >> 3;
    const int c_wait = layer * C_LAYER + (AFTER_G2 ? C_P2 : C_P4) + panel;
    const int c_sig = layer * C_LAYER + (AFTER_G2 ? C_XN2 + (panel >> 1) : C_XN4 + (X.stripe >> 2));
    const int prod = AFTER_G2 ? (panel & 1) * 16 + X.j : (X.j & 7);
    const int u = lane >> 5;
    u32x2 xi[5];
    u32x2* xl = (u32x2*)(shm + LDS_X) + tn * 320 + lane;
    if (FROM_X0) {
#pragma unroll
        for (int j = 0; j < 5; ++j) xi[j] = *(const u32x2*)(p.x0 + ((size_t)X.row * D_ + X.col[j]) * 2);
    } else {
#pragma unroll
        for (int j = 0; j < 5; ++j) xi[j] = xl[j * 64];
    }
    __amdgpu_buffer_rsrc_t rp = rsrc(part, 4 * XB);
    unsigned po0 = ((X.stripe >> 1) * 4 + tn) * 9216 + X.lam * 16, po1 = po0 + 8192 + (X.stripe & 1) * 8;
    po0 += (X.stripe & 1) * 4096 + u * 1024;
    asm volatile("" : "+v"(po0), "+v"(po1));
    if (tn == 0) poll<16>(p, epoch, c_wait, X.j, lane);
    bar();
    u32x4 pa[4], pb[4];
    u32x2 pc[4];
#pragma unroll
    for (int k = 0; k < 4; ++k) {
        pa[k] = ld16(rp, po0, k * (unsigned)XB);
        pb[k] = ld16(rp, po0 + 2048, k * (unsigned)XB);
        pc[k] = ld8(rp, po1, k * (unsigned)XB);
    }
    float v[5][4];
#pragma unroll
    for (int j = 0; j < 5; ++j) {
        float y0 = 0.f, y1 = 0.f, y2 = 0.f, y3 = 0.f;
#pragma unroll
        for (int k = 0; k < 4; ++k) {
            unsigned lo = j == 0 ? pa[k].x : j == 1 ? pa[k].z : j == 2 ? pb[k].x : j == 3 ? pb[k].z : pc[k].x;
            unsigned hi = j == 0 ? pa[k].y : j == 1 ? pa[k].w : j == 2 ? pb[k].y : j == 3 ? pb[k].w : pc[k].y;
            y0 += bf_lo(lo); y1 += bf_hi(lo); y2 += bf_lo(hi); y3 += bf_hi(hi);
        }
        u32x2 o;
        o.x = pack_bf16(bf_lo(xi[j].x) + y0, bf_hi(xi[j].x) + y1);
        o.y = pack_bf16(bf_lo(xi[j].y) + y2, bf_hi(xi[j].y) + y3);
        if (FINAL) xi[j] = o;
        else xl[j * 64] = o;
        v[j][0] = bf_lo(o.x); v[j][1] = bf_hi(o.x); v[j][2] = bf_lo(o.y); v[j][3] = bf_hi(o.y);
    }
    const bool st2 = X.has4 && !((lane >> 3) & 1);
    if (FINAL) {
        u32x4 ch[3];
        int ccol[3];
        x_chunks(X, xi, lane, ch, ccol);
#pragma unroll
        for (int c = 0; c < 3; ++c)
            if (c < 2 || st2) *(u32x4*)(p.out + ((size_t)X.row * D_ + ccol[c]) * 2) = ch[c];
        if (blockIdx.x == 0 && tn == 0 && lane == 0) ((unsigned*)(p.ws + WS_CNT))[F_EPOCH] = epoch;
        return;
    }
    float s = 0.f;
#pragma unroll
    for (int j = 0; j < 4; ++j) s += (v[j][0] + v[j][1]) + (v[j][2] + v[j][3]);
    if (X.has4) s += (v[4][0] + v[4][1]) + (v[4][2] + v[4][3]);
    s = sum_lanes_345(s);
    const float mw = s * (1.f / 144.f);
    float ss = 0.f;
#pragma unroll
    for (int j = 0; j < 5; ++j) {
        float t = 0.f;
#pragma unroll
        for (int e = 0; e < 4; ++e) { float d = v[j][e] - mw; t += d * d; }
        if (j < 4 || X.has4) ss += t;
    }
    ss = sum_lanes_345(ss);
    f32x2* stat = (f32x2*)(shm + LDS_STAT);
    if (lane < 8) stat[tn * 8 + lane] = (f32x2){mw, ss};
    wait_lgkm0();
    bar();
    const f32x2 s0 = stat[X.c8], s1 = stat[8 + X.c8], s2 = stat[16 + X.c8], s3 = stat[24 + X.c8];
    const float mean = (s0.x + s1.x + s2.x + s3.x) * 0.25f;
    const float d0 = s0.x - mean, d1 = s1.x - mean, d2 = s2.x - mean, d3 = s3.x - mean;
    const float var = ((s0.y + s1.y) + (s2.y + s3.y) + 144.f * ((d0 * d0 + d1 * d1) + (d2 * d2 + d3 * d3))) * (1.f / D_);
    const float rs = __builtin_amdgcn_rsqf(var + 1e-5f);
    __amdgpu_buffer_rsrc_t ro = rsrc(xn_out, XB);
#pragma unroll
    for (int j = 0; j < 5; ++j) {
        xi[j].x = pack_bf16((v[j][0] - mean) * rs, (v[j][1] - mean) * rs);
        xi[j].y = pack_bf16((v[j][2] - mean) * rs, (v[j][3] - mean) * rs);
    }
    u32x4 ch[3];
    int ccol[3];
    x_chunks(X, xi, lane, ch, ccol);
#pragma unroll
    for (int c = 0; c < 3; ++c) {
        const int d = ccol[c];
        if (c < 2 || st2) st16(ro, (X.stripe * 18 + (d >> 5)) * 1024 + (((d >> 3) & 3) * 16 + X.half * 8 + X.c8) * 16, ch[c]);
    }
    wait_vm<0>();
    bar();
    if (tn == 0) signal(p, epoch, c_sig, prod, lane);
}

template <int NT, bool TYPEB, class LD, class HOOK>
__device__ __forceinline__ void g3_phase(int layer, char* shm, int w4, unsigned epoch, LD& ld, HOOK hook) {
    OPAQUE_S(w4); OPAQUE_S(epoch);
    const int cbase = layer * C_LAYER;
    using C = Cfg<G3>;
    constexpr int BM = C::BM, MT = C::MT, NK = C::NK, ST = C::ST;
    constexpr int MB = BM * 64, STB = MB + 18432;
    constexpr int CNT = TYPEB ? 5 : 4;
    constexpr int NT0 = TYPEB ? 5 : 0;
    int tm, tn, ks;
    tile_map<G3>(tm, tn, ks);
    const int lane = opaque_lane();
    auto issue = [&](int so) {
        if constexpr (TYPEB) ld.issue(so, lane); else ld.issue(so);
    };
    const int q = lane >> 4;
    const int maddr = lds_base() + (w4 * MT) * 1024 + lane * 16;
    const int naddr = lds_base() + MB + (16 * (q >> 1) + 4 * (q & 1) + ((lane >> 2) & 3)) * 288 + NT0 * 32 + (lane & 3) * 8;

    f32x4 acc[2][NT][MT];
#pragma unroll
    for (int x = 0; x < 2; ++x)
#pragma unroll
        for (int n = 0; n < NT; ++n)
#pragma unroll
            for (int m = 0; m < MT; ++m) acc[x][n][m] = (f32x4){0.f, 0.f, 0.f, 0.f};
    bf16x8 fm0[MT], fm1[MT], fn[2][NT];
    auto load_m = [&](bf16x8(&fm)[MT], int so) {
#pragma unroll
        for (int m = 0; m < MT; ++m) fm[m] = lds16(so + maddr + m * 1024);
    };
    constexpr bool ROLL = true;
    auto load_n = [&](int so, int x) {
#pragma unroll
        for (int n = 0; n < NT; ++n) fn[x][n] = read_n<2>(so + naddr + x * 9216, n);
    };
    constexpr int NMF = 2 * NT * MT;
    auto mma = [&](bf16x8(&fm)[MT], bf16x8(&fnext)[MT], int so_next) {
        __builtin_amdgcn_s_setprio(1);
        load_m(fnext, so_next);
#pragma unroll
        for (int n = 0; n < NT; ++n)
#pragma unroll
            for (int m = 0; m < MT; ++m)
                acc[0][n][m] = __builtin_amdgcn_mfma_f32_16x16x32_bf16(fn[0][n], fm[m], acc[0][n][m], 0, 0, 0);
#pragma unroll
        for (int i = 0; i < MT; ++i) {
            __builtin_amdgcn_sched_group_barrier(0x008, 4, 0);
            __builtin_amdgcn_sched_group_barrier(0x100, 1, 0);
        }
        __builtin_amdgcn_sched_group_barrier(0x008, NMF / 2 - 4 * MT, 0);
        __builtin_amdgcn_sched_barrier(0);
        if (ROLL) load_n(so_next, 0);
#pragma unroll
        for (int n = 0; n < NT; ++n)
#pragma unroll
            for (int m = 0; m < MT; ++m)
                acc[1][n][m] = __builtin_amdgcn_mfma_f32_16x16x32_bf16(fn[1][n], fm[m], acc[1][n][m], 0, 0, 0);
        if (ROLL) {
#pragma unroll
            for (int i = 0; i < 2 * NT; ++i) {
                __builtin_amdgcn_sched_group_barrier(0x008, 1, 1);
                __builtin_amdgcn_sched_group_barrier(0x100, 1, 1);
            }
            __builtin_amdgcn_sched_group_barrier(0x008, NMF / 2 - 2 * NT, 1);
        }
        __builtin_amdgcn_s_setprio(0);
    };

    if constexpr (!TYPEB) {
        const Params p = load_params();
        ld.init(p, WL(p, layer), w4, lane);
        if (w4 == 0) poll<32>(p, epoch, cbase + C_XN2 + tm, tn, lane);
    }
    bar();
    if constexpr (!TYPEB) {
#pragma unroll
        for (int s = 0; s < ST - 1; ++s) issue(s * STB);
    }
    wait_vm<TYPEB ? CNT*(ST - 3) : CNT*(ST - 2)>();
    bar();
    if (TYPEB) bar();
    int rd = 0, wr = (ST - 1) * STB;
    auto stage_wait = [&](int j) {
        if (TYPEB) {
            if (j < NK - (ST - 1)) wait_vm<CNT*(ST - 3)>();
            else wait_vm<0>();
        } else if (j < NK - (ST - 1)) wait_vm<CNT*(ST - 2)>();
        else if (j == NK - (ST - 1)) wait_vm<CNT*(ST - 3)>();
        else wait_vm<0>();
    };
    auto load_phase = [&](int j) {
        if (!ROLL) load_n(rd, 0);
        load_n(rd, 1);
        if (j < NK - (ST - 1)) issue(wr);
        stage_wait(j);
        wr = rd;
        rd = (rd + STB == ST * STB) ? 0 : rd + STB;
        wait_lgkm0();
    };
    load_m(fm0, 0);
    if (ROLL) load_n(0, 0);
    if constexpr (!TYPEB) {
#pragma clang loop unroll(disable)
        for (int j = 0; j < NK; j += 2) {
            load_phase(j);
            bar();
            mma(fm0, fm1, rd);
            bar();
            load_phase(j + 1);
            bar();
            mma(fm1, fm0, rd);
            bar();
        }
    } else {
        load_phase(0);
#pragma clang loop unroll(disable)
        for (int j = 1; j < NK - 1; j += 2) {
            bar();
            mma(fm0, fm1, rd);
            bar();
            load_phase(j);
            bar();
            mma(fm1, fm0, rd);
            bar();
            load_phase(j + 1);
        }
        bar();
        mma(fm0, fm1, rd);
        bar();
        load_phase(NK - 1);
        bar();
        mma(fm1, fm0, rd);
    }
    __builtin_amdgcn_sched_barrier(0);

    OPAQUE_S(w4); OPAQUE_S(epoch);
    const Params p = load_params();
    char* wl = WL(p, layer);
    tile_map<G3>(tm, tn, ks);
    const int l2 = opaque_lane();
    const int r2 = l2 & 15, q2 = l2 >> 4;
    __amdgpu_buffer_rsrc_t ro = rsrc(wl + W_H * XB, 8 * XB);
    float nlog2e = -1.44269504089f;
    OPAQUE_S(nlog2e);
#pragma unroll
    for (int n = 0; n < NT; ++n) {
        const int f16 = tn * 9 + NT0 + n;
        const unsigned ob = ((tm * (BM / 16) + w4 * MT + (q2 & 1)) * 144 + (f16 >> 1)) * 1024 + ((2 * (f16 & 1) + (q2 >> 1)) * 16 + r2) * 16;
#pragma unroll
        for (int m = 0; m < MT; m += 2) {
            u32x2 t[2];
#pragma unroll
            for (int mm = 0; mm < 2; ++mm) {
                const f32x4 a = acc[0][n][m + mm] * nlog2e;
                f32x4 e, r;
#pragma unroll
                for (int j = 0; j < 4; ++j) e[j] = __builtin_amdgcn_exp2f(a[j]);
                e = e + 1.f;
#pragma unroll
                for (int j = 0; j < 4; ++j) r[j] = __builtin_amdgcn_rcpf(e[j]);
                t[mm] = pack4(acc[1][n][m + mm] * r);
            }
            st16(ro, ob + m * (144 * 1024), merge16(t[0], t[1]));
        }
    }
    wait_vm<0>();
    bar();
    if (!TYPEB && w4 == 0) signal(p, epoch, cbase + C_H + tm * 4 + (tn >> 3), tn & 7, l2);
    if (TYPEB) hook();
}

__global__ __launch_bounds__(512, 2) void mega_k(Params p_unused) {
    extern __shared__ __attribute__((aligned(16))) char shm[];
    const int wid = __builtin_amdgcn_readfirstlane(threadIdx.x >> 6);
    const int w4 = wid & 3;
    G1Loader l1;
    if (wid >= 4) l1.prefetch<2>(w4);
    else {
        {
            unsigned long long pc;
            int ea, eb;
            asm volatile("s_getpc_b64 %0\n.Lmega_ref:\n\ts_mov_b32 %1, .Lmega_endA-.Lmega_ref\n\ts_mov_b32 %2, .Lmega_endB-.Lmega_ref" : "=s"(pc), "=s"(ea), "=s"(eb));
            const int size = ea > eb ? ea : eb;
            const int t = (((blockIdx.x >> 3) & 7) * 256 + threadIdx.x) * 64;
            touch((const char*)pc, t + 4 <= size ? t : 0);
        }
    }
    ln_input(shm, wid);
    if (wid < 4) {
        const unsigned epoch = ((const __attribute__((address_space(4))) unsigned*)(load_params().ws + WS_CNT))[F_EPOCH] + 1u;
        for (int layer = 0; layer < 2; ++layer) {
            pc_compute<G1>(layer, shm, w4, epoch);
            pc_compute<G2>(layer, shm, w4, epoch);
            if (layer == 0) x_phase<true, true, false>(layer, shm, w4, epoch);
            else x_phase<true, false, false>(layer, shm, w4, epoch);
            MLoad<G3, 4> ml;
            g3_phase<5, false>(layer, shm, w4, epoch, ml, []() {});
            pc_compute<G4>(layer, shm, w4, epoch);
            if (layer == 0) x_phase<false, false, false>(layer, shm, w4, epoch);
            else x_phase<false, false, true>(layer, shm, w4, epoch);
        }
        asm volatile(".Lmega_endA:");
    } else {
        for (int layer = 0; layer < 2; ++layer) {
            l1.run(layer, shm, w4);
            PcLoader<G2> l2;
            bar();
            l2.prefetch(layer, w4);
            l2.run(layer, w4);
            NLoad<G3> l3;
            auto pf3 = [&]() {
                const Params p = load_params();
                const int lane = opaque_lane();
                l3.init(p, WL(p, layer), w4, lane);
#pragma unroll
                for (int s = 0; s < Cfg<G3>::ST - 1; ++s) l3.issue(s * 34816, lane);
            };
            bar();
            pf3();
            bar();
            bar();
            bar();
            PcLoader<G4> l4;
            g3_phase<4, true>(layer, shm, w4, 0u, l3, [&]() { l4.prefetch(layer, w4); });
            l4.run(layer, w4);
            bar();
            if (layer == 0) l1.prefetch<4>(w4);
            bar();
            if (layer == 0) {
                bar();
                bar();
            }
        }
        asm volatile(".Lmega_endB:");
    }
}

extern "C" void kernel_launch(void* const* d_in, const int* in_sizes, int n_in, void* d_out, int out_size, void* d_ws,
                              size_t ws_size, hipStream_t stream) {
    Params p;
    p.x0 = (const char*)d_in[0];
    p.Cr = (const char*)d_in[1];
    p.Ci = (const char*)d_in[2];
    p.Wr = (const char*)d_in[3];
    p.Wi = (const char*)d_in[4];
    p.Wg = (const char*)d_in[6];
    p.Wu = (const char*)d_in[7];
    p.Wd = (const char*)d_in[8];
    p.out = (char*)d_out;
    constexpr size_t need = WS_CNT + (size_t)(F_EPOCH + 64) * 4;
    p.ws = (char*)d_ws + ((ws_size - need) & ~(size_t)((2u << 20) - 1));
    (void)hipFuncSetAttribute((const void*)mega_k, hipFuncAttributeMaxDynamicSharedMemorySize, LDS_TOTAL);
    mega_k<<<256, 512, LDS_TOTAL, stream>>>(p);
}
```
